```python
import torch

def _softmax(x, axis=-1):
    return torch.softmax(x, dim=axis)

def _gelu(x):
    return torch.nn.functional.gelu(x, approximate='tanh')

def _silu(x):
    return torch.nn.functional.silu(x)

def _sigmoid(x):
    return torch.sigmoid(x)

def _layernorm(x, axis=-1, eps=1e-05):
    m = x.mean(dim=axis, keepdim=True)
    v = x.var(dim=axis, keepdim=True, unbiased=False)
    return (x - m) / torch.sqrt(v + eps)

def _scatter_add(tgt, idx, src, axis=0):
    return tgt.clone().index_add_(axis, idx, src)

def _segment_sum(src, seg, n):
    out = torch.zeros((n,) + src.shape[1:], dtype=src.dtype)
    return out.index_add_(0, seg, src)

def _pad(x, pw):
    flat = tuple((v for pair in reversed(pw) for v in pair))
    return torch.nn.functional.pad(x, flat)
kernel_name = 'sequential_preLN__memory_aug_recurrent_mem_tr__mixer_channel_mlp__prompt'

def setup_inputs():

    _w = lambda a: (lambda t: t.to(torch.bfloat16) if t.is_floating_point() else t)(torch.as_tensor(a))

    def _A_setup():
        B, S, Dm = (1, 2048, 640)
        H, Dh = (32, 192)
        M, seg = (8, 32)
        seg = min(seg, S)
        S = S // seg * seg
        return {'x': _w((lambda *s: torch.randn(*s, dtype=torch.bfloat16) * 0.05)(B, S, Dm)), 'mem0': _w((lambda *s: torch.randn(*s, dtype=torch.bfloat16) * 0.2)(B, M, Dm)), 'Wq': _w((lambda *s: torch.randn(*s, dtype=torch.bfloat16) * 1.6)(Dm, H, Dh) * Dm ** (-0.5)), 'Wk': _w((lambda *s: torch.randn(*s, dtype=torch.bfloat16) * 1.6)(Dm, H, Dh) * Dm ** (-0.5)), 'Wv': _w((lambda *s: torch.randn(*s, dtype=torch.bfloat16) * 0.2)(Dm, H, Dh) * Dm ** (-0.5)), 'Wo': _w((lambda *s: torch.randn(*s, dtype=torch.bfloat16) * 0.2)(H, Dh, Dm) * (H * Dh) ** (-0.5)), 'seg_len': seg}
    _A_in = _A_setup()

    def _B_setup():
        B, S, D, F = (1, 2048, 640, 1536)
        return {'x': _w((lambda *s: torch.randn(*s, dtype=torch.bfloat16) * 0.05)(B, S, D)), 'w1': _w((lambda *s: torch.randn(*s, dtype=torch.bfloat16) * 0.8)(D, F) * D ** (-0.5)), 'b1': _w((lambda *s: torch.randn(*s, dtype=torch.bfloat16) * 0.4)(F)), 'w2': _w((lambda *s: torch.randn(*s, dtype=torch.bfloat16) * 0.05)(F, D) * F ** (-0.5)), 'b2': _w((lambda *s: torch.randn(*s, dtype=torch.bfloat16) * 0.05)(D))}
    _B_in = _B_setup()
    return {'A_x': _A_in['x'], 'A_mem0': _A_in['mem0'], 'A_Wq': _A_in['Wq'], 'A_Wk': _A_in['Wk'], 'A_Wv': _A_in['Wv'], 'A_Wo': _A_in['Wo'], 'A_seg_len': _A_in['seg_len'], 'B_x': _B_in['x'], 'B_w1': _B_in['w1'], 'B_b1': _B_in['b1'], 'B_w2': _B_in['w2'], 'B_b2': _B_in['b2']}

def reference(A_x, A_mem0, A_Wq, A_Wk, A_Wv, A_Wo, A_seg_len, B_x, B_w1, B_b1, B_w2, B_b2):

    def _A_ref(x, mem0, Wq, Wk, Wv, Wo, seg_len):
        B, S, Dm = x.shape
        H, Dh = (Wq.shape[1], Wq.shape[2])
        M = mem0.shape[1]
        scale = Dh ** (-0.5)
        n_seg = S // seg_len
        L = M + seg_len
        idx = torch.arange(L)
        mask = (idx[:, None] < M) | (idx[:, None] >= idx[None, :])
        mem = mem0
        outs = []
        for s in range(n_seg):
            xs = x[:, s * seg_len:(s + 1) * seg_len, :].clone()
            h = torch.concatenate([mem, xs], axis=1)
            q = torch.einsum('btd,dhe->bhte', h, Wq)
            k = torch.einsum('btd,dhe->bhte', h, Wk)
            v = torch.einsum('btd,dhe->bhte', h, Wv)
            scores = torch.einsum('bhqe,bhke->bhqk', q, k) * scale
            scores = torch.where(mask[None, None], scores, -1000000000.0)
            p = _softmax(scores, axis=-1)
            o = torch.einsum('bhqk,bhke->bhqe', p, v)
            o = torch.einsum('bhte,hed->btd', o, Wo)
            mem = o[:, :M, :].clone()
            outs.append(o[:, M:, :])
        return torch.concatenate(outs, axis=1)

    def _B_ref(x, w1, b1, w2, b2):
        y = _layernorm(x, axis=-1)
        h = _gelu(torch.einsum('bsd,df->bsf', y, w1) + b1)
        return torch.einsum('bsf,fd->bsd', h, w2) + b2
    x = A_x
    h = x + _A_ref(_layernorm(x, axis=-1), A_mem0, A_Wq, A_Wk, A_Wv, A_Wo, A_seg_len)
    h2 = h + _B_ref(_layernorm(h, axis=-1), B_w1, B_b1, B_w2, B_b2)
    return h2
```

```cpp
#include <hip/hip_runtime.h>
#include <hip/hip_bf16.h>

typedef unsigned short u16;
typedef unsigned long long u64;
using bf16x8 = __attribute__((ext_vector_type(8))) short;
using f32x4 = __attribute__((ext_vector_type(4))) float;
typedef unsigned v4u __attribute__((ext_vector_type(4)));
typedef unsigned v2u __attribute__((ext_vector_type(2)));

#define NSEG 64
#define DM 640
#define NH 32
#define DH 192
#define HD 6144
#define QLD 18432
#define NTOK 2048
#define FF 1536

__device__ __forceinline__ float bf2f(u16 v) { return __uint_as_float(((unsigned)v) << 16); }
__device__ __forceinline__ u16 f2bf(float f) {
    __hip_bfloat16 h = __float2bfloat16(f);
    return *reinterpret_cast<u16*>(&h);
}
__device__ __forceinline__ float bfr(float f) { return bf2f(f2bf(f)); }
__device__ __forceinline__ unsigned pack2(float a, float b) { return (unsigned)f2bf(a) | ((unsigned)f2bf(b) << 16); }

__global__ __launch_bounds__(256) void k_transpose(const u16* __restrict__ in, u16* __restrict__ out, int R, int C,
                                                   long in_bs, long out_bs) {
    __shared__ u16 t[64][66];
    const u16* ib = in + (long)blockIdx.z * in_bs;
    u16* ob = out + (long)blockIdx.z * out_bs;
    int c0 = blockIdx.x * 64, r0 = blockIdx.y * 64;
    int tx = threadIdx.x & 63, ty = threadIdx.x >> 6;
    for (int i = ty; i < 64; i += 4) t[i][tx] = ib[(long)(r0 + i) * C + c0 + tx];
    __syncthreads();
    for (int i = ty; i < 64; i += 4) ob[(long)(c0 + i) * R + r0 + tx] = t[tx][i];
}

__device__ __forceinline__ float wave_sum(float v) {
#pragma unroll
    for (int m = 32; m >= 1; m >>= 1) v += __shfl_xor(v, m);
    return v;
}
__device__ __forceinline__ void ln_row(float (&v)[10]) {
    float s = 0.f;
#pragma unroll
    for (int i = 0; i < 10; ++i) s += v[i];
    s = wave_sum(s);
    float mean = s * (1.0f / DM);
    float q = 0.f;
#pragma unroll
    for (int i = 0; i < 10; ++i) { float d = v[i] - mean; q += d * d; }
    q = wave_sum(q);
    float mb = bfr(mean);
    float vb = bfr(q * (1.0f / DM));
    float den = bfr(sqrtf(bfr(vb + 1e-5f)));
#pragma unroll
    for (int i = 0; i < 10; ++i) v[i] = bfr(bfr(v[i] - mb) / den);
}
__global__ __launch_bounds__(256) void k_ln(const u16* __restrict__ in, u16* __restrict__ out, int twice) {
    int row = blockIdx.x * 4 + (threadIdx.x >> 6), lane = threadIdx.x & 63;
    float v[10];
#pragma unroll
    for (int i = 0; i < 10; ++i) v[i] = bf2f(in[(long)row * DM + lane + 64 * i]);
    ln_row(v);
    if (twice) ln_row(v);
#pragma unroll
    for (int i = 0; i < 10; ++i) out[(long)row * DM + lane + 64 * i] = f2bf(v[i]);
}

enum { EPI_QKV = 0, EPI_UT = 1, EPI_MLP1 = 2, EPI_MLP2 = 3 };
struct GemmArgs {
    const u16* A; const u16* Bt; u16* C;
    const u16* bias; const u16* res;
    long lda, ldb, sA, sB;
    int K, pad0;
};
__device__ __forceinline__ float gelu_tanh(float x) {
    float u = 0.7978845608028654f * (x + 0.044715f * x * x * x);
    return 0.5f * x * (1.0f + tanhf(u));
}
template <int EPI>
__global__ __launch_bounds__(256, 2) void k_gemm(GemmArgs g) {
    __shared__ __attribute__((aligned(16))) u16 sm[2][2][128 * 32];
    const int bz = blockIdx.z;
    const u16* Ag = g.A + (long)bz * g.sA + (long)blockIdx.y * 128 * g.lda;
    const u16* Bg = g.Bt + (long)bz * g.sB + (long)blockIdx.x * 128 * g.ldb;
    const int tid = threadIdx.x, wid = tid >> 6, lane = tid & 63, wr = wid >> 1, wc = wid & 1, fr = lane & 15, fq = lane >> 4;
    auto stage = [&](int kt, int buf) {
#pragma unroll
        for (int i = 0; i < 2; ++i) {
            int b = tid * 16 + i * 4096, r = b >> 6, c = (b & 63) >> 1;
            __builtin_amdgcn_global_load_lds((const unsigned*)(Ag + (long)r * g.lda + kt * 32 + c),
                                             (unsigned*)((char*)&sm[buf][0][0] + b), 16, 0, 0);
            __builtin_amdgcn_global_load_lds((const unsigned*)(Bg + (long)r * g.ldb + kt * 32 + c),
                                             (unsigned*)((char*)&sm[buf][1][0] + b), 16, 0, 0);
        }
    };
    f32x4 acc[4][4];
#pragma unroll
    for (int m = 0; m < 4; ++m)
#pragma unroll
        for (int n = 0; n < 4; ++n) acc[m][n] = (f32x4){0.f, 0.f, 0.f, 0.f};
    const int nt = g.K >> 5;
    stage(0, 0);
    asm volatile("s_waitcnt vmcnt(0)" ::: "memory");
    __syncthreads();
    for (int t = 0; t < nt; ++t) {
        int cur = t & 1;
        if (t + 1 < nt) stage(t + 1, cur ^ 1);
        bf16x8 af[4], bfg[4];
#pragma unroll
        for (int m = 0; m < 4; ++m) af[m] = *reinterpret_cast<const bf16x8*>(&sm[cur][0][(wr * 64 + m * 16 + fr) * 32 + fq * 8]);
#pragma unroll
        for (int n = 0; n < 4; ++n) bfg[n] = *reinterpret_cast<const bf16x8*>(&sm[cur][1][(wc * 64 + n * 16 + fr) * 32 + fq * 8]);
#pragma unroll
        for (int m = 0; m < 4; ++m)
#pragma unroll
            for (int n = 0; n < 4; ++n) acc[m][n] = __builtin_amdgcn_mfma_f32_16x16x32_bf16(af[m], bfg[n], acc[m][n], 0, 0, 0);
        asm volatile("s_waitcnt vmcnt(0)" ::: "memory");
        __syncthreads();
    }
    const int row0 = blockIdx.y * 128 + wr * 64 + fq * 4, col0 = blockIdx.x * 128 + wc * 64 + fr;
#pragma unroll
    for (int m = 0; m < 4; ++m)
#pragma unroll
        for (int n = 0; n < 4; ++n) {
            const int row = row0 + m * 16, col = col0 + n * 16;
            if constexpr (EPI == EPI_QKV) {
#pragma unroll
                for (int j = 0; j < 4; ++j) g.C[(long)(row + j) * QLD + col] = f2bf(acc[m][n][j]);
            } else if constexpr (EPI == EPI_UT) {
                v2u o; o.x = pack2(acc[m][n][0], acc[m][n][1]); o.y = pack2(acc[m][n][2], acc[m][n][3]);
                *reinterpret_cast<v2u*>(g.C + ((long)(row >> 5) * DM + col) * 1024 + bz * 32 + (row & 31)) = o;
            } else if constexpr (EPI == EPI_MLP1) {
                float b = bf2f(g.bias[col]);
#pragma unroll
                for (int j = 0; j < 4; ++j) {
                    float t = bfr(bfr(acc[m][n][j]) + b);
                    g.C[(long)(row + j) * FF + col] = f2bf(gelu_tanh(t));
                }
            } else {
                float b = bf2f(g.bias[col]);
#pragma unroll
                for (int j = 0; j < 4; ++j) {
                    float t = bfr(bfr(acc[m][n][j]) + b);
                    g.C[(long)(row + j) * DM + col] = f2bf(bf2f(g.res[(long)(row + j) * DM + col]) + t);
                }
            }
        }
}

#define NA 32
#define NB 32
#define NC 40
#define SPIN_CAP 4000000u

struct ChainArgs {
    const u16* mem0; const u16* WT; const u16* WoT; const u16* QKV; const u16* UT;
    u16* km; u16* ZT;
    u64* memg;
    u64* pg;
    u64* zg;
    unsigned* ctl;
};

#define MEM_LD 648
#define QK_LD 200
#define NLDSF 17
#define L_W 0
#define L_MEM (136 * 1024)
#define L_QK (L_MEM + 10368)
#define L_SC (L_QK + 6400)
#define L_P (L_SC + 1568)
#define L_END (L_P + 672)
static_assert(L_END <= 160 * 1024, "lds");
#define C_PLD 1288
#define LC_P 0
#define LC_Z (LC_P + 20608)
#define LC_RED (LC_Z + 8448)

__device__ __forceinline__ bf16x8 ld_frag(const u16* p) { return *reinterpret_cast<const bf16x8*>(p); }

__device__ __forceinline__ void st_sc1_x4(void* p, v4u v) {
    asm volatile("global_store_dwordx4 %0, %1, off sc1\n\ts_nop 1" ::"v"(p), "v"(v) : "memory");
}
__device__ __forceinline__ bool abort_seen(unsigned* ctl) {
    return __builtin_amdgcn_readfirstlane(__hip_atomic_load(ctl, __ATOMIC_RELAXED, __HIP_MEMORY_SCOPE_AGENT)) != 0u;
}

__device__ __forceinline__ bool gather_mem(const ChainArgs& a, char* smem, int s, int tid) {
    u16* mimg = reinterpret_cast<u16*>(smem + L_MEM);
    if (s == 0) {
        for (int i = tid; i < 8 * 80; i += 512) {
            int m = i / 80, c = (i % 80) * 8;
            *reinterpret_cast<bf16x8*>(mimg + m * MEM_LD + c) = ld_frag(a.mem0 + m * DM + c);
        }
        return true;
    }
    const u64* base = a.memg + (size_t)(s & 1) * (NC * 64);
    const int i0 = tid, i1 = tid + 512, i2 = (tid < 256) ? tid + 1024 : tid;
    const u64* p0 = base + 2 * i0; const u64* p1 = base + 2 * i1; const u64* p2 = base + 2 * i2;
    v4u r0, r1, r2;
    unsigned spins = 0;
    const unsigned tag = (unsigned)s;
    for (;;) {
        asm volatile("global_load_dwordx4 %0, %3, off sc1\n\tglobal_load_dwordx4 %1, %4, off sc1\n\t"
                     "global_load_dwordx4 %2, %5, off sc1\n\ts_waitcnt vmcnt(0)"
                     : "=&v"(r0), "=&v"(r1), "=&v"(r2) : "v"(p0), "v"(p1), "v"(p2) : "memory");
        bool ok = (r0.y == tag) & (r0.w == tag) & (r1.y == tag) & (r1.w == tag) & (r2.y == tag) & (r2.w == tag);
        if (__all(ok)) break;
        __builtin_amdgcn_s_sleep(1);
        if (++spins > SPIN_CAP) { if ((tid & 63) == 0) __hip_atomic_store(a.ctl, 1u, __ATOMIC_RELAXED, __HIP_MEMORY_SCOPE_AGENT); return false; }
        if ((spins & 1023u) == 0u && abort_seen(a.ctl)) return false;
    }
    auto put = [&](int idx, v4u r) {
        int c = idx >> 5, w = idx & 31, m = w >> 2, g = w & 3;
        v2u o; o.x = r.x; o.y = r.z;
        *reinterpret_cast<v2u*>(mimg + m * MEM_LD + c * 16 + g * 4) = o;
    };
    put(i0, r0); put(i1, r1);
    if (tid < 256) put(i2, r2);
    return true;
}

template <int NREG, bool KX>
__device__ __forceinline__ void role_a(const ChainArgs& a, char* smem, int h, int wave, int lds_off) {
    const int tid = threadIdx.x, lane = tid & 63, fr = lane & 15, fq = lane >> 4;
    bf16x8 wr[NREG];
    char* lw = smem + L_W + lds_off + lane * 16;
#pragma unroll
    for (int f = 0; f < 60; ++f) {
        const int t = f / 20, ks = f % 20, tile = 3 * wave + t;
        const int nrow = (tile < 12 ? h * DH + tile * 16 : HD + h * DH + (tile - 12) * 16) + fr;
        bf16x8 v = ld_frag(a.WT + (long)nrow * DM + ks * 32 + fq * 8);
        if (f < NREG) wr[f] = v; else *reinterpret_cast<bf16x8*>(lw + (f - NREG) * 1024) = v;
    }
    u16* mimg = reinterpret_cast<u16*>(smem + L_MEM);
    u16* qk = reinterpret_cast<u16*>(smem + L_QK);
    float* sc = reinterpret_cast<float*>(smem + L_SC);
    u16* pimg = reinterpret_cast<u16*>(smem + L_P);
    if (tid < 16) pimg[(tid >> 1) * 42 + 40 + (tid & 1)] = 0;
    bf16x8 kx[6];
    auto kx_load = [&](int s) {
        const u16* kp = a.QKV + (long)(s * 32 + (wave - 1) * 16 + fr) * QLD + HD + h * DH + fq * 8;
#pragma unroll
        for (int ks = 0; ks < 6; ++ks) kx[ks] = ld_frag(kp + ks * 32);
    };
    if constexpr (KX) kx_load(0);
    const float scale = 0.07216878364870322f;
    for (int s = 0; s < NSEG; ++s) {
        if (!gather_mem(a, smem, s, tid)) return;
        __syncthreads();
        f32x4 acc[3];
#pragma unroll
        for (int t = 0; t < 3; ++t) acc[t] = (f32x4){0.f, 0.f, 0.f, 0.f};
        const u16* mrow = mimg + (lane & 7) * MEM_LD + fq * 8;
#pragma unroll
        for (int ks = 0; ks < 20; ++ks) {
            bf16x8 mf = *reinterpret_cast<const bf16x8*>(mrow + ks * 32);
#pragma unroll
            for (int t = 0; t < 3; ++t) {
                const int f = t * 20 + ks;
                bf16x8 w;
                if (f < NREG) w = wr[f]; else w = *reinterpret_cast<const bf16x8*>(lw + (f - NREG) * 1024);
                acc[t] = __builtin_amdgcn_mfma_f32_16x16x32_bf16(w, mf, acc[t], 0, 0, 0);
            }
        }
        if (fr < 8) {
#pragma unroll
            for (int t = 0; t < 3; ++t) {
                const int tile = 3 * wave + t;
                v2u o; o.x = pack2(acc[t][0], acc[t][1]); o.y = pack2(acc[t][2], acc[t][3]);
                if (tile < 12) {
                    *reinterpret_cast<v2u*>(qk + fr * QK_LD + tile * 16 + fq * 4) = o;
                } else {
                    *reinterpret_cast<v2u*>(qk + (8 + fr) * QK_LD + (tile - 12) * 16 + fq * 4) = o;
                    *reinterpret_cast<v2u*>(a.km + (((long)s * NH + h) * 8 + fr) * DH + (tile - 12) * 16 + fq * 4) = o;
                }
            }
        }
        __syncthreads();
        if (s + 1 < NSEG) {
            if (wave < 3) {
                f32x4 sa = (f32x4){0.f, 0.f, 0.f, 0.f};
                const u16* qrow = qk + (lane & 7) * QK_LD + fq * 8;
                const u16* krow = qk + (8 + (lane & 7)) * QK_LD + fq * 8;
#pragma unroll
                for (int ks = 0; ks < 6; ++ks) {
                    bf16x8 qf = *reinterpret_cast<const bf16x8*>(qrow + ks * 32);
                    bf16x8 kf;
                    if constexpr (!KX) kf = *reinterpret_cast<const bf16x8*>(krow + ks * 32); else kf = kx[ks];
                    sa = __builtin_amdgcn_mfma_f32_16x16x32_bf16(kf, qf, sa, 0, 0, 0);
                }
                if (fr < 8) {
                    if constexpr (!KX) {
                        if (fq < 2) {
#pragma unroll
                            for (int r = 0; r < 4; ++r) sc[fr * 49 + fq * 4 + r] = sa[r];
                        }
                    } else {
#pragma unroll
                        for (int r = 0; r < 4; ++r) sc[fr * 49 + 8 + (wave - 1) * 16 + fq * 4 + r] = sa[r];
                    }
                }
                if constexpr (KX) kx_load(s + 1);
            }
            __syncthreads();
            if (!KX && wave == 0) {
                const int i = lane >> 3, sub = lane & 7;
                float sv[5], mx = -3.0e38f;
#pragma unroll
                for (int j = 0; j < 5; ++j) { sv[j] = bfr(bfr(sc[i * 49 + sub * 5 + j]) * scale); mx = fmaxf(mx, sv[j]); }
                mx = fmaxf(mx, __shfl_xor(mx, 1)); mx = fmaxf(mx, __shfl_xor(mx, 2)); mx = fmaxf(mx, __shfl_xor(mx, 4));
                float sum = 0.f;
#pragma unroll
                for (int j = 0; j < 5; ++j) { sv[j] = __expf(sv[j] - mx); sum += sv[j]; }
                sum += __shfl_xor(sum, 1); sum += __shfl_xor(sum, 2); sum += __shfl_xor(sum, 4);
                const float inv = 1.0f / sum;
#pragma unroll
                for (int j = 0; j < 5; ++j) pimg[i * 42 + sub * 5 + j] = f2bf(sv[j] * inv);
                asm volatile("s_waitcnt lgkmcnt(0)" ::: "memory");
                __builtin_amdgcn_wave_barrier();
                if (lane < 56) {
                    const int ri = lane / 7, pr = lane % 7;
                    const u16* pp = pimg + ri * 42 + pr * 6;
                    const unsigned tg = (unsigned)(s + 1) << 16;
                    v4u o;
                    o.x = (unsigned)pp[0] | ((unsigned)pp[1] << 16); o.y = (unsigned)pp[2] | tg;
                    o.z = (unsigned)pp[3] | ((unsigned)pp[4] << 16); o.w = (unsigned)pp[5] | tg;
                    st_sc1_x4(a.pg + ((size_t)(s & 1) * NA + h) * 112 + ri * 14 + pr * 2, o);
                }
            }
        }
    }
}

template <int T1, int T2, int NL>
__device__ __forceinline__ void role_b_wave(const ChainArgs& a, char* smem, int h, int wave, int lane, int t1_0, int t2_0, int lds_off) {
    const int tid = threadIdx.x, fr = lane & 15, fq = lane >> 4;
    constexpr int NF = T1 * 20 + T2 * 6, NREG = NF - NL;
    bf16x8 wr[NREG];
    char* lw = smem + L_W + lds_off + lane * 16;
#pragma unroll
    for (int f = 0; f < NF; ++f) {
        bf16x8 v;
        if (f < T1 * 20) {
            const int t = f / 20, ks = f % 20;
            v = ld_frag(a.WT + (long)(2 * HD + h * DH + (t1_0 + t) * 16 + fr) * DM + ks * 32 + fq * 8);
        } else {
            const int f2 = f - T1 * 20, t = f2 / 6, ks = f2 % 6;
            v = ld_frag(a.WoT + ((long)h * DM + (t2_0 + t) * 16 + fr) * DH + ks * 32 + fq * 8);
        }
        if (f < NREG) wr[f] = v; else *reinterpret_cast<bf16x8*>(lw + (f - NREG) * 1024) = v;
    }
    u16* mimg = reinterpret_cast<u16*>(smem + L_MEM);
    u16* vimg = reinterpret_cast<u16*>(smem + L_QK);
    for (int s = 0; s < NSEG; ++s) {
        if (!gather_mem(a, smem, s, tid)) return;
        __syncthreads();
        {
            f32x4 acc[T1];
#pragma unroll
            for (int t = 0; t < T1; ++t) acc[t] = (f32x4){0.f, 0.f, 0.f, 0.f};
            const u16* mrow = mimg + (lane & 7) * MEM_LD + fq * 8;
#pragma unroll
            for (int ks = 0; ks < 20; ++ks) {
                bf16x8 mf = *reinterpret_cast<const bf16x8*>(mrow + ks * 32);
#pragma unroll
                for (int t = 0; t < T1; ++t) {
                    const int f = t * 20 + ks;
                    bf16x8 w;
                    if (f < NREG) w = wr[f]; else w = *reinterpret_cast<const bf16x8*>(lw + (f - NREG) * 1024);
                    acc[t] = __builtin_amdgcn_mfma_f32_16x16x32_bf16(w, mf, acc[t], 0, 0, 0);
                }
            }
            if (fr < 8) {
#pragma unroll
                for (int t = 0; t < T1; ++t) {
                    v2u o; o.x = pack2(acc[t][0], acc[t][1]); o.y = pack2(acc[t][2], acc[t][3]);
                    *reinterpret_cast<v2u*>(vimg + fr * QK_LD + (t1_0 + t) * 16 + fq * 4) = o;
                }
            }
        }
        __syncthreads();
        {
            f32x4 acc[T2];
#pragma unroll
            for (int t = 0; t < T2; ++t) acc[t] = (f32x4){0.f, 0.f, 0.f, 0.f};
            const u16* vrow = vimg + (lane & 7) * QK_LD + fq * 8;
#pragma unroll
            for (int ks = 0; ks < 6; ++ks) {
                bf16x8 vf = *reinterpret_cast<const bf16x8*>(vrow + ks * 32);
#pragma unroll
                for (int t = 0; t < T2; ++t) {
                    const int f = T1 * 20 + t * 6 + ks;
                    bf16x8 w;
                    if (f < NREG) w = wr[f]; else w = *reinterpret_cast<const bf16x8*>(lw + (f - NREG) * 1024);
                    acc[t] = __builtin_amdgcn_mfma_f32_16x16x32_bf16(w, vf, acc[t], 0, 0, 0);
                }
            }
            if (fr < 8) {
#pragma unroll
                for (int t = 0; t < T2; ++t) {
                    const int ct = t2_0 + t;
                    const unsigned lo = pack2(acc[t][0], acc[t][1]), hi = pack2(acc[t][2], acc[t][3]);
                    if (s + 1 < NSEG) {
                        v4u o; o.x = lo; o.y = (unsigned)(s + 1); o.z = hi; o.w = (unsigned)(s + 1);
                        st_sc1_x4(a.zg + (((size_t)(s & 1) * NC + ct) * NB + h) * 64 + (fr * 4 + fq) * 2, o);
                    }
                    u16* zt = a.ZT + ((long)s * DM + ct * 16 + fq * 4) * 256 + h * 8 + fr;
                    zt[0] = (u16)(lo & 0xffff); zt[256] = (u16)(lo >> 16); zt[512] = (u16)(hi & 0xffff); zt[768] = (u16)(hi >> 16);
                }
            }
        }
    }
}

__device__ __forceinline__ void role_c(const ChainArgs& a, char* smem, int c) {
    const int tid = threadIdx.x, wave = __builtin_amdgcn_readfirstlane(tid >> 6), lane = tid & 63, fr = lane & 15, fq = lane >> 4;
    u16* pimg = reinterpret_cast<u16*>(smem + LC_P);
    u16* zimg = reinterpret_cast<u16*>(smem + LC_Z);
    float* red = reinterpret_cast<float*>(smem + LC_RED);
    int pdst[4]; int pidx[4];
#pragma unroll
    for (int r = 0; r < 4; ++r) {
        int idx = tid + 512 * r; if (idx >= 1792) idx = tid;
        pidx[r] = idx;
        int hh = idx / 56, rem = idx % 56, i = rem / 7, pr = rem % 7;
        pdst[r] = (hh << 16) | (i << 8) | pr;
    }
    bf16x8 uf[4];
    auto u_load = [&](int s) {
        const u16* up = a.UT + ((long)s * DM + c * 16 + fr) * 1024 + wave * 128 + fq * 8;
#pragma unroll
        for (int k = 0; k < 4; ++k) uf[k] = ld_frag(up + k * 32);
    };
    u_load(0);
    for (int s = 0; s + 1 < NSEG; ++s) {
        const u64* pb = a.pg + (size_t)(s & 1) * (NA * 112);
        const u64* zb = a.zg + ((size_t)(s & 1) * NC + c) * (NB * 64);
        const u64 *q0 = pb + 2 * pidx[0], *q1 = pb + 2 * pidx[1], *q2 = pb + 2 * pidx[2], *q3 = pb + 2 * pidx[3];
        const u64 *z0 = zb + 2 * tid, *z1 = zb + 2 * (tid + 512);
        v4u r0, r1, r2, r3, y0, y1;
        const unsigned tag = (unsigned)(s + 1);
        unsigned spins = 0;
        for (;;) {
            asm volatile("global_load_dwordx4 %0, %6, off sc1\n\tglobal_load_dwordx4 %1, %7, off sc1\n\t"
                         "global_load_dwordx4 %2, %8, off sc1\n\tglobal_load_dwordx4 %3, %9, off sc1\n\t"
                         "global_load_dwordx4 %4, %10, off sc1\n\tglobal_load_dwordx4 %5, %11, off sc1\n\ts_waitcnt vmcnt(0)"
                         : "=&v"(r0), "=&v"(r1), "=&v"(r2), "=&v"(r3), "=&v"(y0), "=&v"(y1)
                         : "v"(q0), "v"(q1), "v"(q2), "v"(q3), "v"(z0), "v"(z1) : "memory");
            bool ok = ((r0.y >> 16) == tag) & ((r0.w >> 16) == tag) & ((r1.y >> 16) == tag) & ((r1.w >> 16) == tag) &
                      ((r2.y >> 16) == tag) & ((r2.w >> 16) == tag) & ((r3.y >> 16) == tag) & ((r3.w >> 16) == tag) &
                      (y0.y == tag) & (y0.w == tag) & (y1.y == tag) & (y1.w == tag);
            if (__all(ok)) break;
            __builtin_amdgcn_s_sleep(1);
            if (++spins > SPIN_CAP) { if (lane == 0) __hip_atomic_store(a.ctl, 1u, __ATOMIC_RELAXED, __HIP_MEMORY_SCOPE_AGENT); return; }
            if ((spins & 1023u) == 0u && abort_seen(a.ctl)) return;
        }
        auto putp = [&](int d, v4u r) {
            const int hh = d >> 16, i = (d >> 8) & 255, pr = d & 255;
            u16 v[6] = {(u16)(r.x & 0xffff), (u16)(r.x >> 16), (u16)(r.y & 0xffff), (u16)(r.z & 0xffff), (u16)(r.z >> 16), (u16)(r.w & 0xffff)};
#pragma unroll
            for (int j = 0; j < 6; ++j) {
                const int slot = pr * 6 + j;
                if (slot < 40) {
                    const int k = slot < 8 ? 1024 + hh * 8 + slot : hh * 32 + slot - 8;
                    pimg[i * C_PLD + k] = v[j];
                }
            }
        };
        putp(pdst[0], r0); putp(pdst[1], r1); putp(pdst[2], r2); putp(pdst[3], r3);
        auto putz = [&](int idx, v4u r) {
            const int hh = idx >> 5, w = idx & 31, m = w >> 2, g = w & 3;
            u16* z = zimg + (g * 4) * 264 + hh * 8 + m;
            z[0] = (u16)(r.x & 0xffff); z[264] = (u16)(r.x >> 16); z[528] = (u16)(r.z & 0xffff); z[792] = (u16)(r.z >> 16);
        };
        putz(tid, y0); putz(tid + 512, y1);
        __syncthreads();
        f32x4 acc = (f32x4){0.f, 0.f, 0.f, 0.f};
        const u16* prow = pimg + (lane & 7) * C_PLD + fq * 8;
#pragma unroll
        for (int k = 0; k < 4; ++k) {
            bf16x8 pf = *reinterpret_cast<const bf16x8*>(prow + wave * 128 + k * 32);
            acc = __builtin_amdgcn_mfma_f32_16x16x32_bf16(uf[k], pf, acc, 0, 0, 0);
        }
        {
            bf16x8 pf = *reinterpret_cast<const bf16x8*>(prow + 1024 + wave * 32);
            bf16x8 zf = *reinterpret_cast<const bf16x8*>(zimg + fr * 264 + wave * 32 + fq * 8);
            acc = __builtin_amdgcn_mfma_f32_16x16x32_bf16(zf, pf, acc, 0, 0, 0);
        }
        *reinterpret_cast<f32x4*>(red + (wave * 64 + lane) * 4) = acc;
        if (s + 2 < NSEG) u_load(s + 1);
        __syncthreads();
        if (wave == 0 && fr < 8) {
            f32x4 t = *reinterpret_cast<const f32x4*>(red + lane * 4);
#pragma unroll
            for (int w = 1; w < 8; ++w) {
                f32x4 u = *reinterpret_cast<const f32x4*>(red + (w * 64 + lane) * 4);
                t[0] += u[0]; t[1] += u[1]; t[2] += u[2]; t[3] += u[3];
            }
            v4u o; o.x = pack2(t[0], t[1]); o.y = tag; o.z = pack2(t[2], t[3]); o.w = tag;
            st_sc1_x4(a.memg + ((size_t)((s + 1) & 1) * NC + c) * 64 + (fr * 4 + fq) * 2, o);
        }
    }
}

__global__ __launch_bounds__(512, 2) void k_chain(ChainArgs a) {
    __shared__ __attribute__((aligned(16))) char smem[160 * 1024];
    const int b = blockIdx.x;
    if (b < NA) {
        const int wave = __builtin_amdgcn_readfirstlane(threadIdx.x >> 6);
        if (wave == 1 || wave == 2) role_a<37, true>(a, smem, b, wave, (15 + (wave - 1) * 23) * 1024);
        else role_a<45, false>(a, smem, b, wave, (wave == 0 ? 0 : 61 + (wave - 3) * 15) * 1024);
    } else if (b < NA + NB) {
        const int h = b - NA;
        const int wave = __builtin_amdgcn_readfirstlane(threadIdx.x >> 6), lane = threadIdx.x & 63;
        if (wave < 4) role_b_wave<2, 4, 21>(a, smem, h, wave, lane, 2 * wave, 4 * wave, wave * 21 * 1024);
        else role_b_wave<1, 6, 13>(a, smem, h, wave, lane, 8 + (wave - 4), 16 + 6 * (wave - 4), (84 + (wave - 4) * 13) * 1024);
    } else {
        role_c(a, smem, b - NA - NB);
    }
}

#define XP_LD 1288
__global__ __launch_bounds__(512, 2) void k_xattn(const u16* __restrict__ QKV, const u16* __restrict__ km,
                                                  const u16* __restrict__ UT, const u16* __restrict__ ZT,
                                                  const u16* __restrict__ x, u16* __restrict__ hout) {
    __shared__ __attribute__((aligned(16))) u16 P[32 * XP_LD];
    const int seg = blockIdx.y, cb = blockIdx.x;
    const int tid = threadIdx.x, wave = tid >> 6, lane = tid & 63, fr = lane & 15, fq = lane >> 4;
    const float scale = 0.07216878364870322f;
    for (int hh = 0; hh < 4; ++hh) {
        const int h = wave * 4 + hh;
        const u16* kmp = km + (((long)seg * NH + h) * 8 + (fr & 7)) * DH + fq * 8;
        const u16* kx0 = QKV + (long)(seg * 32 + fr) * QLD + HD + h * DH + fq * 8;
        const u16* kx1 = kx0 + (long)16 * QLD;
#pragma unroll
        for (int qt = 0; qt < 2; ++qt) {
            const u16* qp = QKV + (long)(seg * 32 + qt * 16 + fr) * QLD + h * DH + fq * 8;
            f32x4 s0 = (f32x4){0.f, 0.f, 0.f, 0.f}, s1 = s0, s2 = s0;
#pragma unroll
            for (int ks = 0; ks < 6; ++ks) {
                bf16x8 qf = ld_frag(qp + ks * 32);
                s0 = __builtin_amdgcn_mfma_f32_16x16x32_bf16(ld_frag(kmp + ks * 32), qf, s0, 0, 0, 0);
                s1 = __builtin_amdgcn_mfma_f32_16x16x32_bf16(ld_frag(kx0 + ks * 32), qf, s1, 0, 0, 0);
                s2 = __builtin_amdgcn_mfma_f32_16x16x32_bf16(ld_frag(kx1 + ks * 32), qf, s2, 0, 0, 0);
            }
            const int ql = qt * 16 + fr;
            float v[12]; float mx = -3.0e38f;
#pragma unroll
            for (int r = 0; r < 4; ++r) {
                v[r] = (fq < 2) ? bfr(bfr(s0[r]) * scale) : -1.0e9f;
                v[4 + r] = (fq * 4 + r <= ql) ? bfr(bfr(s1[r]) * scale) : -1.0e9f;
                v[8 + r] = (16 + fq * 4 + r <= ql) ? bfr(bfr(s2[r]) * scale) : -1.0e9f;
            }
#pragma unroll
            for (int j = 0; j < 12; ++j) mx = fmaxf(mx, v[j]);
            mx = fmaxf(mx, __shfl_xor(mx, 16)); mx = fmaxf(mx, __shfl_xor(mx, 32));
            float sum = 0.f;
#pragma unroll
            for (int j = 0; j < 12; ++j) { v[j] = __expf(v[j] - mx); sum += v[j]; }
            if (fq >= 2) { sum -= v[0] + v[1] + v[2] + v[3]; }
            sum += __shfl_xor(sum, 16); sum += __shfl_xor(sum, 32);
            const float inv = 1.0f / sum;
            u16* prow = P + ql * XP_LD;
            if (fq < 2) {
                v2u o; o.x = pack2(v[0] * inv, v[1] * inv); o.y = pack2(v[2] * inv, v[3] * inv);
                *reinterpret_cast<v2u*>(prow + 1024 + h * 8 + fq * 4) = o;
            }
            {
                v2u o; o.x = pack2(v[4] * inv, v[5] * inv); o.y = pack2(v[6] * inv, v[7] * inv);
                *reinterpret_cast<v2u*>(prow + h * 32 + fq * 4) = o;
                o.x = pack2(v[8] * inv, v[9] * inv); o.y = pack2(v[10] * inv, v[11] * inv);
                *reinterpret_cast<v2u*>(prow + h * 32 + 16 + fq * 4) = o;
            }
        }
    }
    __syncthreads();
    const int col0 = cb * 128 + wave * 16;
    f32x4 a0 = (f32x4){0.f, 0.f, 0.f, 0.f}, a1 = a0;
    const u16* up = UT + ((long)seg * DM + col0 + fr) * 1024 + fq * 8;
    const u16* zp = ZT + ((long)seg * DM + col0 + fr) * 256 + fq * 8;
    const u16* p0 = P + fr * XP_LD + fq * 8;
    const u16* p1 = P + (16 + fr) * XP_LD + fq * 8;
#pragma unroll 8
    for (int ks = 0; ks < 32; ++ks) {
        bf16x8 uf = ld_frag(up + ks * 32);
        a0 = __builtin_amdgcn_mfma_f32_16x16x32_bf16(uf, *reinterpret_cast<const bf16x8*>(p0 + ks * 32), a0, 0, 0, 0);
        a1 = __builtin_amdgcn_mfma_f32_16x16x32_bf16(uf, *reinterpret_cast<const bf16x8*>(p1 + ks * 32), a1, 0, 0, 0);
    }
#pragma unroll
    for (int ks = 0; ks < 8; ++ks) {
        bf16x8 zf = ld_frag(zp + ks * 32);
        a0 = __builtin_amdgcn_mfma_f32_16x16x32_bf16(zf, *reinterpret_cast<const bf16x8*>(p0 + 1024 + ks * 32), a0, 0, 0, 0);
        a1 = __builtin_amdgcn_mfma_f32_16x16x32_bf16(zf, *reinterpret_cast<const bf16x8*>(p1 + 1024 + ks * 32), a1, 0, 0, 0);
    }
#pragma unroll
    for (int qt = 0; qt < 2; ++qt) {
        const f32x4 av = qt ? a1 : a0;
        const long off = (long)(seg * 32 + qt * 16 + fr) * DM + col0 + fq * 4;
        v2u xi = *reinterpret_cast<const v2u*>(x + off);
        v2u o;
        o.x = pack2(bf2f((u16)(xi.x & 0xffff)) + bfr(av[0]), bf2f((u16)(xi.x >> 16)) + bfr(av[1]));
        o.y = pack2(bf2f((u16)(xi.y & 0xffff)) + bfr(av[2]), bf2f((u16)(xi.y >> 16)) + bfr(av[3]));
        *reinterpret_cast<v2u*>(hout + off) = o;
    }
}

extern "C" void kernel_launch(void* const* d_in, const int* in_sizes, int n_in, void* d_out, int out_size, void* d_ws,
                              size_t ws_size, hipStream_t stream) {
    const u16* x = (const u16*)d_in[0];
    const u16* mem0 = (const u16*)d_in[1];
    const u16* Wq = (const u16*)d_in[2];
    const u16* Wk = (const u16*)d_in[3];
    const u16* Wv = (const u16*)d_in[4];
    const u16* Wo = (const u16*)d_in[5];
    const u16* w1 = (const u16*)d_in[8];
    const u16* b1 = (const u16*)d_in[9];
    const u16* w2 = (const u16*)d_in[10];
    const u16* b2 = (const u16*)d_in[11];
    u16* out = (u16*)d_out;

    char* ws = (char*)d_ws;
    size_t off = 0;
    auto take = [&](size_t bytes) { char* p = ws + off; off += (bytes + 255) & ~(size_t)255; return p; };
    const size_t comm_bytes = (size_t)(2 * NC * 64 + 2 * NA * 112 + 2 * NC * NB * 64) * 8 + 256;
    char* comm = take(comm_bytes);
    u16* xn = (u16*)take((size_t)NTOK * DM * 2);
    u16* WT = (u16*)take((size_t)QLD * DM * 2);
    u16* WoT = (u16*)take((size_t)NH * DM * DH * 2);
    u16* w1T = (u16*)take((size_t)FF * DM * 2);
    u16* w2T = (u16*)take((size_t)DM * FF * 2);
    u16* QKV = (u16*)take((size_t)NTOK * QLD * 2);
    u16* UT = (u16*)take((size_t)NSEG * DM * 1024 * 2);
    u16* km = (u16*)take((size_t)NSEG * NH * 8 * DH * 2);
    u16* ZT = (u16*)take((size_t)NSEG * DM * 256 * 2);
    u16* hbuf = (u16*)take((size_t)NTOK * DM * 2);
    u16* ybuf = (u16*)take((size_t)NTOK * DM * 2);
    u16* hid = (u16*)take((size_t)NTOK * FF * 2);

    hipMemsetAsync(comm, 0, comm_bytes, stream);
    k_transpose<<<dim3(HD / 64, DM / 64, 1), 256, 0, stream>>>(Wq, WT, DM, HD, 0, 0);
    k_transpose<<<dim3(HD / 64, DM / 64, 1), 256, 0, stream>>>(Wk, WT + (size_t)HD * DM, DM, HD, 0, 0);
    k_transpose<<<dim3(HD / 64, DM / 64, 1), 256, 0, stream>>>(Wv, WT + (size_t)2 * HD * DM, DM, HD, 0, 0);
    k_transpose<<<dim3(DM / 64, DH / 64, NH), 256, 0, stream>>>(Wo, WoT, DH, DM, (long)DH * DM, (long)DM * DH);
    k_transpose<<<dim3(FF / 64, DM / 64, 1), 256, 0, stream>>>(w1, w1T, DM, FF, 0, 0);
    k_transpose<<<dim3(DM / 64, FF / 64, 1), 256, 0, stream>>>(w2, w2T, FF, DM, 0, 0);
    k_ln<<<NTOK / 4, 256, 0, stream>>>(x, xn, 0);
    {
        GemmArgs g{xn, WT, QKV, nullptr, nullptr, DM, DM, 0, 0, DM, 0};
        k_gemm<EPI_QKV><<<dim3(QLD / 128, NTOK / 128, 1), 256, 0, stream>>>(g);
    }
    {
        GemmArgs g{QKV + 2 * HD, WoT, UT, nullptr, nullptr, QLD, DH, DH, (long)DM * DH, DH, 0};
        k_gemm<EPI_UT><<<dim3(DM / 128, NTOK / 128, NH), 256, 0, stream>>>(g);
    }
    {
        ChainArgs a;
        a.mem0 = mem0; a.WT = WT; a.WoT = WoT; a.QKV = QKV; a.UT = UT; a.km = km; a.ZT = ZT;
        a.memg = (u64*)comm; a.pg = a.memg + 2 * NC * 64; a.zg = a.pg + 2 * NA * 112;
        a.ctl = (unsigned*)(a.zg + 2 * NC * NB * 64);
        k_chain<<<NA + NB + NC, 512, 0, stream>>>(a);
    }
    k_xattn<<<dim3(DM / 128, NSEG), 512, 0, stream>>>(QKV, km, UT, ZT, x, hbuf);
    k_ln<<<NTOK / 4, 256, 0, stream>>>(hbuf, ybuf, 1);
    {
        GemmArgs g{ybuf, w1T, hid, b1, nullptr, DM, DM, 0, 0, DM, 0};
        k_gemm<EPI_MLP1><<<dim3(FF / 128, NTOK / 128, 1), 256, 0, stream>>>(g);
    }
    {
        GemmArgs g{hid, w2T, out, b2, hbuf, FF, FF, 0, 0, FF, 0};
        k_gemm<EPI_MLP2><<<dim3(DM / 128, NTOK / 128, 1), 256, 0, stream>>>(g);
    }
}
```

```cpp
#include <hip/hip_runtime.h>
#include <hip/hip_bf16.h>

typedef unsigned short u16;
typedef unsigned long long u64;
using bf16x8 = __attribute__((ext_vector_type(8))) short;
using f32x4 = __attribute__((ext_vector_type(4))) float;
typedef unsigned v4u __attribute__((ext_vector_type(4)));
typedef unsigned v2u __attribute__((ext_vector_type(2)));

#define NSEG 64
#define DM 640
#define NH 32
#define DH 192
#define HD 6144
#define QLD 12288
#define NTOK 2048
#define FF 1536

__device__ __forceinline__ float bf2f(u16 v) { return __uint_as_float(((unsigned)v) << 16); }
__device__ __forceinline__ u16 f2bf(float f) {
    __hip_bfloat16 h = __float2bfloat16(f);
    return *reinterpret_cast<u16*>(&h);
}
__device__ __forceinline__ float bfr(float f) { return bf2f(f2bf(f)); }
__device__ __forceinline__ unsigned pack2(float a, float b) { return (unsigned)f2bf(a) | ((unsigned)f2bf(b) << 16); }
__device__ __forceinline__ bf16x8 ld_frag(const u16* p) { return *reinterpret_cast<const bf16x8*>(p); }
#define MFMA(a, b, c) __builtin_amdgcn_mfma_f32_16x16x32_bf16((a), (b), (c), 0, 0, 0)
__device__ __forceinline__ float max4g(float x) {
    auto r = __builtin_amdgcn_permlane16_swap(__float_as_uint(x), __float_as_uint(x), false, false);
    x = fmaxf(__uint_as_float(r[0]), __uint_as_float(r[1]));
    auto q = __builtin_amdgcn_permlane32_swap(__float_as_uint(x), __float_as_uint(x), false, false);
    return fmaxf(__uint_as_float(q[0]), __uint_as_float(q[1]));
}
__device__ __forceinline__ float sum4g(float x) {
    auto r = __builtin_amdgcn_permlane16_swap(__float_as_uint(x), __float_as_uint(x), false, false);
    x = __uint_as_float(r[0]) + __uint_as_float(r[1]);
    auto q = __builtin_amdgcn_permlane32_swap(__float_as_uint(x), __float_as_uint(x), false, false);
    return __uint_as_float(q[0]) + __uint_as_float(q[1]);
}

__device__ __forceinline__ float wave_sum(float v) {
#pragma unroll
    for (int m = 32; m >= 1; m >>= 1) v += __shfl_xor(v, m);
    return v;
}
__device__ __forceinline__ void ln_row(float (&v)[10]) {
    float s = 0.f;
#pragma unroll
    for (int i = 0; i < 10; ++i) s += v[i];
    s = wave_sum(s);
    float mean = s * (1.0f / DM);
    float q = 0.f;
#pragma unroll
    for (int i = 0; i < 10; ++i) { float d = v[i] - mean; q += d * d; }
    q = wave_sum(q);
    float mb = bfr(mean);
    float vb = bfr(q * (1.0f / DM));
    float den = bfr(sqrtf(bfr(vb + 1e-5f)));
#pragma unroll
    for (int i = 0; i < 10; ++i) v[i] = bfr(bfr(v[i] - mb) / den);
}

template <int R>
__device__ __forceinline__ void ln_rows(float (&v)[R][10]) {
    float s[R], q[R];
#pragma unroll
    for (int r = 0; r < R; ++r) {
        s[r] = 0.f;
#pragma unroll
        for (int i = 0; i < 10; ++i) s[r] += v[r][i];
    }
#pragma unroll
    for (int m = 32; m >= 1; m >>= 1)
#pragma unroll
        for (int r = 0; r < R; ++r) s[r] += __shfl_xor(s[r], m);
#pragma unroll
    for (int r = 0; r < R; ++r) {
        s[r] *= (1.0f / DM);
        q[r] = 0.f;
#pragma unroll
        for (int i = 0; i < 10; ++i) { float d = v[r][i] - s[r]; q[r] += d * d; }
    }
#pragma unroll
    for (int m = 32; m >= 1; m >>= 1)
#pragma unroll
        for (int r = 0; r < R; ++r) q[r] += __shfl_xor(q[r], m);
#pragma unroll
    for (int r = 0; r < R; ++r) {
        const float mb = bfr(s[r]);
        const float den = bfr(sqrtf(bfr(bfr(q[r] * (1.0f / DM)) + 1e-5f)));
        const float rden = 1.0f / den;
#pragma unroll
        for (int i = 0; i < 10; ++i) v[r][i] = bfr(bfr(v[r][i] - mb) * rden);
    }
}

struct PreArgs {
    const u16 *Wq, *Wk, *Wv, *Wo, *w1, *w2, *x;
    u16 *WT, *WoT, *w1T, *w2T, *xn;
};
__device__ __forceinline__ void tr_tile(const u16* ib, u16* ob, int R, int C, int r0, int c0, u16 (*t)[66]) {
    int tx = threadIdx.x & 63, ty = threadIdx.x >> 6;
    for (int i = ty; i < 64; i += 4) t[i][tx] = ib[(long)(r0 + i) * C + c0 + tx];
    __syncthreads();
    for (int i = ty; i < 64; i += 4) ob[(long)(c0 + i) * R + r0 + tx] = t[tx][i];
}
__global__ __launch_bounds__(256) void k_pre(PreArgs p) {
    __shared__ u16 t[64][66];
    int b = blockIdx.x;
    if (b < 512) {
        int row = b * 4 + (threadIdx.x >> 6), lane = threadIdx.x & 63;
        float v[10];
#pragma unroll
        for (int i = 0; i < 10; ++i) v[i] = bf2f(p.x[(long)row * DM + lane + 64 * i]);
        ln_row(v);
#pragma unroll
        for (int i = 0; i < 10; ++i) p.xn[(long)row * DM + lane + 64 * i] = f2bf(v[i]);
        return;
    }
    b -= 512;
    if (b < 2880) {
        int w = b / 960, r = b % 960;
        const u16* src = w == 0 ? p.Wq : (w == 1 ? p.Wk : p.Wv);
        tr_tile(src, p.WT + (size_t)w * HD * DM, DM, HD, (r % 10) * 64, (r / 10) * 64, t);
        return;
    }
    b -= 2880;
    if (b < 960) {
        int h = b / 30, r = b % 30;
        tr_tile(p.Wo + (size_t)h * DH * DM, p.WoT + (size_t)h * DM * DH, DH, DM, (r % 3) * 64, (r / 3) * 64, t);
        return;
    }
    b -= 960;
    if (b < 240) {
        tr_tile(p.w1, p.w1T, DM, FF, (b % 10) * 64, (b / 10) * 64, t);
        return;
    }
    b -= 240;
    tr_tile(p.w2, p.w2T, FF, DM, (b % 24) * 64, (b / 24) * 64, t);
}
#define PRE_BLOCKS (512 + 2880 + 960 + 240 + 240)

#define NA 32
#define NB 32
#define NC 40
#define NCHAIN (NA + NB + NC)
#define SPIN_CAP 3000000u
#define GATE_A 40
#define GATE_B 64
#define GATE_C 64

struct Mega {
    const u16 *x, *mem0, *b1, *b2;
    const u16 *xn, *WT, *WoT, *w1T, *w2T;
    u16 *QK, *UT, *km, *ZT, *hbuf, *hid, *out, *Pbuf;
    v4u* memg;
    v4u* pg;
    v4u* zg;
    unsigned* ctl;
};
#define CW(i) ((i) * 16)
#define C_ABORT 0
#define C_TICKET 1
#define C_K 2
#define C_U 18
#define C_Q 34
#define C_CH 50
#define C_X 114
#define C_M 146
#define C_P 178
#define C_NWORDS 242

#define STAMP(slot) do {} while (0)

#define MEM_LD 648
#define QK_LD 200
#define L_W 0
#define L_KX (128 * 1024)
#define L_MEM (140 * 1024)
#define L_QK (L_MEM + 10368)
#define L_END (L_QK + 6400)
static_assert(L_END <= 160 * 1024 - 64, "lds");
#define C_PLD 1288
#define Z_LD 264
#define LC_P 0
#define LC_Z (LC_P + 20608)
#define LC_RED (LC_Z + 8448)
#define L_BCAST (160 * 1024 - 64)

__device__ __forceinline__ void st_sc1_x4(void* p, v4u v) {
    asm volatile("global_store_dwordx4 %0, %1, off sc1\n\ts_nop 1" ::"v"(p), "v"(v) : "memory");
}
__device__ __forceinline__ void st_sc1_x2(void* p, v2u v) {
    asm volatile("global_store_dwordx2 %0, %1, off sc1\n\ts_nop 0" ::"v"(p), "v"(v) : "memory");
}
__device__ __forceinline__ v4u ld_sc1(__amdgpu_buffer_rsrc_t r, unsigned off) { return __builtin_amdgcn_raw_buffer_load_b128(r, off, 0, 16); }
#define TAGOK(r, tag) (((r).y == (tag)) & ((r).w == (tag)))
__device__ __forceinline__ unsigned ld_ctl(unsigned* p) {
    return (unsigned)__builtin_amdgcn_readfirstlane(__hip_atomic_load(p, __ATOMIC_RELAXED, __HIP_MEMORY_SCOPE_AGENT));
}
__device__ __forceinline__ void set_abort(unsigned* ctl) { __hip_atomic_store(ctl + CW(C_ABORT), 1u, __ATOMIC_RELAXED, __HIP_MEMORY_SCOPE_AGENT); }
__device__ __forceinline__ bool wave_wait(unsigned* ctl, int word, unsigned target) {
    unsigned spins = 0;
    while (ld_ctl(ctl + CW(word)) < target) {
        __builtin_amdgcn_s_sleep(4);
        if (++spins > SPIN_CAP) { set_abort(ctl); return false; }
        if ((spins & 255u) == 0u && ld_ctl(ctl + CW(C_ABORT)) != 0u) return false;
    }
    return true;
}
__device__ __forceinline__ void wg_signal(unsigned* ctl, int word) {
    asm volatile("s_waitcnt vmcnt(0)" ::: "memory");
    __syncthreads();
    if (threadIdx.x == 0) __hip_atomic_fetch_add(ctl + CW(word), 1u, __ATOMIC_RELAXED, __HIP_MEMORY_SCOPE_AGENT);
}

__device__ __forceinline__ bool gather_mem(const Mega& a, char* smem, int s, int tid) {
    u16* mimg = reinterpret_cast<u16*>(smem + L_MEM);
    if (s == 0) {
        for (int i = tid; i < 8 * 80; i += 512) {
            int m = i / 80, c = (i % 80) * 8;
            *reinterpret_cast<bf16x8*>(mimg + m * MEM_LD + c) = ld_frag(a.mem0 + m * DM + c);
        }
        asm volatile("s_waitcnt vmcnt(0)" ::: "memory");
        return true;
    }
    const __amdgpu_buffer_rsrc_t rs = __builtin_amdgcn_make_buffer_rsrc((void*)a.memg, 0, 2 * NC * 32 * 16, 0x00020000);
    const int i0 = tid, i1 = tid + 512, i2 = (tid < 256) ? tid + 1024 : tid;
    const unsigned ob = (unsigned)(s & 1) * (NC * 32 * 16);
    v4u r0 = (v4u){0u, 0u, 0u, 0u}, r1 = r0, r2 = r0;
    unsigned spins = 0;
    const unsigned tag = (unsigned)s;
    for (;;) {
        if (!TAGOK(r0, tag)) r0 = ld_sc1(rs, ob + i0 * 16);
        if (!TAGOK(r1, tag)) r1 = ld_sc1(rs, ob + i1 * 16);
        if (!TAGOK(r2, tag)) r2 = ld_sc1(rs, ob + i2 * 16);
        asm volatile("" ::: "memory");
        bool ok = TAGOK(r0, tag) & TAGOK(r1, tag) & TAGOK(r2, tag);
        if (__all(ok)) break;
        if (++spins > SPIN_CAP) { if ((tid & 63) == 0) set_abort(a.ctl); return false; }
        if ((spins & 1023u) == 0u && ld_ctl(a.ctl + CW(C_ABORT)) != 0u) return false;
    }
    asm volatile("s_waitcnt vmcnt(0)" ::: "memory");
    auto put = [&](int idx, v4u r) {
        int c = idx >> 5, w = idx & 31, m = w >> 2, g = w & 3;
        v2u o; o.x = r.x; o.y = r.z;
        *reinterpret_cast<v2u*>(mimg + m * MEM_LD + c * 16 + g * 4) = o;
    };
    put(i0, r0); put(i1, r1);
    if (tid < 256) put(i2, r2);
    return true;
}

__device__ __forceinline__ void role_a(const Mega& a, char* smem, int h) {
    const int tid = threadIdx.x, wave = __builtin_amdgcn_readfirstlane(tid >> 6), lane = tid & 63, fr = lane & 15, fq = lane >> 4;
    constexpr int NREG = 44;
    bf16x8 wr[NREG];
    char* lw = smem + L_W + wave * (16 * 1024) + lane * 16;
#pragma unroll
    for (int f = 0; f < 60; ++f) {
        const int t = f / 20, ks = f % 20, tile = 3 * wave + t;
        const int nrow = (tile < 12 ? h * DH + tile * 16 : HD + h * DH + (tile - 12) * 16) + fr;
        bf16x8 v = ld_frag(a.WT + (long)nrow * DM + ks * 32 + fq * 8);
        if (f < NREG) wr[f] = v; else *reinterpret_cast<bf16x8*>(lw + (f - NREG) * 1024) = v;
    }
    u16* mimg = reinterpret_cast<u16*>(smem + L_MEM);
    u16* qk = reinterpret_cast<u16*>(smem + L_QK);
    auto kx_load = [&](int s) -> bool {
        if ((s & 3) == 0) { if (!wave_wait(a.ctl, C_K + (s >> 2), 24u)) return false; }
        const u16* kp = a.QK + (long)(s * 32 + fr) * QLD + HD + h * DH + fq * 8;
#pragma unroll
        for (int t = 0; t < 2; ++t)
#pragma unroll
            for (int ks = 0; ks < 6; ++ks)
                __builtin_amdgcn_global_load_lds((const unsigned*)(kp + (long)t * 16 * QLD + ks * 32),
                                                 (unsigned*)(smem + L_KX + (t * 6 + ks) * 1024), 16, 0, 16);
        return true;
    };
    if (wave == 0) kx_load(0);
    const float scale = 0.07216878364870322f;
    constexpr int STAMPW = 0;
    for (int s = 0; s < NSEG; ++s) {
        if (!gather_mem(a, smem, s, tid)) return;
        STAMP(0);
        __syncthreads();
        if (s > 0 && tid == 0) __hip_atomic_fetch_add(a.ctl + CW(C_CH + s - 1), 1u, __ATOMIC_RELAXED, __HIP_MEMORY_SCOPE_AGENT);
        STAMP(1);
        f32x4 acc[3];
#pragma unroll
        for (int t = 0; t < 3; ++t) acc[t] = (f32x4){0.f, 0.f, 0.f, 0.f};
        const u16* mrow = mimg + (lane & 7) * MEM_LD + fq * 8;
#pragma unroll
        for (int ks = 0; ks < 20; ++ks) {
            bf16x8 mf = *reinterpret_cast<const bf16x8*>(mrow + ks * 32);
#pragma unroll
            for (int t = 0; t < 3; ++t) {
                const int f = t * 20 + ks;
                bf16x8 w;
                if (f < NREG) w = wr[f]; else w = *reinterpret_cast<const bf16x8*>(lw + (f - NREG) * 1024);
                acc[t] = MFMA(w, mf, acc[t]);
            }
        }
        if (fr < 8) {
#pragma unroll
            for (int t = 0; t < 3; ++t) {
                const int tile = 3 * wave + t;
                v2u o; o.x = pack2(acc[t][0], acc[t][1]); o.y = pack2(acc[t][2], acc[t][3]);
                if (tile < 12) {
                    *reinterpret_cast<v2u*>(qk + fr * QK_LD + tile * 16 + fq * 4) = o;
                } else {
                    *reinterpret_cast<v2u*>(qk + (8 + fr) * QK_LD + (tile - 12) * 16 + fq * 4) = o;
                    st_sc1_x2(a.km + (((long)s * NH + h) * 8 + fr) * DH + (tile - 12) * 16 + fq * 4, o);
                }
            }
        }
        STAMP(2);
        __syncthreads();
        STAMP(3);
        if (wave == 0 && s + 1 < NSEG) {
            f32x4 s0 = (f32x4){0.f, 0.f, 0.f, 0.f}, s1 = s0, s2 = s0;
            const u16* qrow = qk + (lane & 7) * QK_LD + fq * 8;
            const u16* krow = qk + (8 + (lane & 7)) * QK_LD + fq * 8;
            const char* kxp = smem + L_KX + lane * 16;
#pragma unroll
            for (int ks = 0; ks < 6; ++ks) {
                bf16x8 qf = *reinterpret_cast<const bf16x8*>(qrow + ks * 32);
                s0 = MFMA(*reinterpret_cast<const bf16x8*>(krow + ks * 32), qf, s0);
                s1 = MFMA(*reinterpret_cast<const bf16x8*>(kxp + ks * 1024), qf, s1);
                s2 = MFMA(*reinterpret_cast<const bf16x8*>(kxp + (6 + ks) * 1024), qf, s2);
            }
            float v[12]; float mx = -3.0e38f;
#pragma unroll
            for (int r = 0; r < 4; ++r) {
                v[r] = (fq < 2) ? bfr(bfr(s0[r]) * scale) : -1.0e9f;
                v[4 + r] = bfr(bfr(s1[r]) * scale);
                v[8 + r] = bfr(bfr(s2[r]) * scale);
            }
#pragma unroll
            for (int j = 0; j < 12; ++j) mx = fmaxf(mx, v[j]);
            mx = max4g(mx);
            float sum = 0.f;
#pragma unroll
            for (int j = 0; j < 12; ++j) { v[j] = (j < 4 && fq >= 2) ? 0.f : __expf(v[j] - mx); sum += v[j]; }
            sum = sum4g(sum);
            const float inv = 1.0f / sum;
            if (fr < 8) {
                v4u* pb = a.pg + ((size_t)(s & 1) * NA + h) * 80 + fr * 10;
                const unsigned tg = (unsigned)(s + 1);
                v4u o; o.y = tg; o.w = tg;
                o.x = pack2(v[4] * inv, v[5] * inv); o.z = pack2(v[6] * inv, v[7] * inv);
                st_sc1_x4(pb + 2 + fq, o);
                o.x = pack2(v[8] * inv, v[9] * inv); o.z = pack2(v[10] * inv, v[11] * inv);
                st_sc1_x4(pb + 6 + fq, o);
                if (fq < 2) {
                    o.x = pack2(v[0] * inv, v[1] * inv); o.z = pack2(v[2] * inv, v[3] * inv);
                    st_sc1_x4(pb + fq, o);
                }
            }
            STAMP(4);
            asm volatile("s_waitcnt lgkmcnt(0)" ::: "memory");
            kx_load(s + 1);
        }
        __builtin_amdgcn_s_barrier();
    }
    wg_signal(a.ctl, C_CH + NSEG - 1);
}

template <int T1, int T2, int NL>
__device__ __forceinline__ void role_b_wave(const Mega& a, char* smem, int h, int lane, int t1_0, int t2_0, int lds_off) {
    const int tid = threadIdx.x, fr = lane & 15, fq = lane >> 4;
    constexpr int NF = T1 * 20 + T2 * 6, NREG = NF - NL;
    bf16x8 wr[NREG];
    char* lw = smem + L_W + lds_off + lane * 16;
#pragma unroll
    for (int f = 0; f < NF; ++f) {
        bf16x8 v;
        if (f < T1 * 20) {
            const int t = f / 20, ks = f % 20;
            v = ld_frag(a.WT + (long)(2 * HD + h * DH + (t1_0 + t) * 16 + fr) * DM + ks * 32 + fq * 8);
        } else {
            const int f2 = f - T1 * 20, t = f2 / 6, ks = f2 % 6;
            v = ld_frag(a.WoT + ((long)h * DM + (t2_0 + t) * 16 + fr) * DH + ks * 32 + fq * 8);
        }
        if (f < NREG) wr[f] = v; else *reinterpret_cast<bf16x8*>(lw + (f - NREG) * 1024) = v;
    }
    u16* mimg = reinterpret_cast<u16*>(smem + L_MEM);
    u16* vimg = reinterpret_cast<u16*>(smem + L_QK);
    for (int s = 0; s < NSEG; ++s) {
        if (!gather_mem(a, smem, s, tid)) return;
        __syncthreads();
        {
            f32x4 acc[T1];
#pragma unroll
            for (int t = 0; t < T1; ++t) acc[t] = (f32x4){0.f, 0.f, 0.f, 0.f};
            const u16* mrow = mimg + (lane & 7) * MEM_LD + fq * 8;
#pragma unroll
            for (int ks = 0; ks < 20; ++ks) {
                bf16x8 mf = *reinterpret_cast<const bf16x8*>(mrow + ks * 32);
#pragma unroll
                for (int t = 0; t < T1; ++t) {
                    const int f = t * 20 + ks;
                    bf16x8 w;
                    if (f < NREG) w = wr[f]; else w = *reinterpret_cast<const bf16x8*>(lw + (f - NREG) * 1024);
                    acc[t] = MFMA(w, mf, acc[t]);
                }
            }
            if (fr < 8) {
#pragma unroll
                for (int t = 0; t < T1; ++t) {
                    v2u o; o.x = pack2(acc[t][0], acc[t][1]); o.y = pack2(acc[t][2], acc[t][3]);
                    *reinterpret_cast<v2u*>(vimg + fr * QK_LD + (t1_0 + t) * 16 + fq * 4) = o;
                }
            }
        }
        __syncthreads();
        {
            f32x4 acc[T2];
#pragma unroll
            for (int t = 0; t < T2; ++t) acc[t] = (f32x4){0.f, 0.f, 0.f, 0.f};
            const u16* vrow = vimg + (lane & 7) * QK_LD + fq * 8;
#pragma unroll
            for (int ks = 0; ks < 6; ++ks) {
                bf16x8 vf = *reinterpret_cast<const bf16x8*>(vrow + ks * 32);
#pragma unroll
                for (int t = 0; t < T2; ++t) {
                    const int f = T1 * 20 + t * 6 + ks;
                    bf16x8 w;
                    if (f < NREG) w = wr[f]; else w = *reinterpret_cast<const bf16x8*>(lw + (f - NREG) * 1024);
                    acc[t] = MFMA(vf, w, acc[t]);
                }
            }
            if (fq < 2) {
#pragma unroll
                for (int t = 0; t < T2; ++t) {
                    v4u o; o.x = pack2(acc[t][0], acc[t][1]); o.y = (unsigned)(s + 1); o.z = pack2(acc[t][2], acc[t][3]); o.w = (unsigned)(s + 1);
                    st_sc1_x4(a.zg + (((size_t)(s & 1) * NC + t2_0 + t) * NB + h) * 32 + fr * 2 + fq, o);
                }
            }
        }
    }
}

__device__ __forceinline__ void role_c(const Mega& a, char* smem, int c) {
    const int tid = threadIdx.x, wave = __builtin_amdgcn_readfirstlane(tid >> 6), lane = tid & 63, fr = lane & 15, fq = lane >> 4;
    u16* pimg = reinterpret_cast<u16*>(smem + LC_P);
    u16* zimg = reinterpret_cast<u16*>(smem + LC_Z);
    float* red = reinterpret_cast<float*>(smem + LC_RED);
    int pdst[5];
#pragma unroll
    for (int r = 0; r < 5; ++r) {
        const int idx = tid + 512 * r;
        const int hh = idx / 80, rem = idx % 80, i = rem / 10, j = rem % 10;
        pdst[r] = i * C_PLD + (j < 2 ? 1024 + hh * 8 + 4 * j : hh * 32 + 4 * (j - 2));
    }
    int zdst[2];
#pragma unroll
    for (int r = 0; r < 2; ++r) {
        const int idx = tid + 512 * r;
        const int hh = idx >> 5, w = idx & 31;
        zdst[r] = (w >> 1) * Z_LD + hh * 8 + (w & 1) * 4;
    }
    const __amdgpu_buffer_rsrc_t urs = __builtin_amdgcn_make_buffer_rsrc((void*)a.UT, 0, NSEG * NH * DM * 32 * 2, 0x00020000);
    bf16x8 uf[4];
    auto u_load = [&](int s) -> bool {
        if ((s & 3) == 0) { if (!wave_wait(a.ctl, C_U + (s >> 2), 32u)) return false; }
#pragma unroll
        for (int k = 0; k < 4; ++k) {
            const unsigned off = (unsigned)((((s * NH + wave * 4 + k) * DM + c * 16 + fr) * 32 + fq * 8) * 2);
            v4u t = ld_sc1(urs, off);
            uf[k] = __builtin_bit_cast(bf16x8, t);
        }
        return true;
    };
    if (!u_load(0)) return;
    const __amdgpu_buffer_rsrc_t prs = __builtin_amdgcn_make_buffer_rsrc((void*)a.pg, 0, 2 * NA * 80 * 16, 0x00020000);
    const __amdgpu_buffer_rsrc_t zrs = __builtin_amdgcn_make_buffer_rsrc((void*)a.zg, 0, 2 * NC * NB * 32 * 16, 0x00020000);
    constexpr int STAMPW = 0;
    for (int s = 0; s < NSEG; ++s) {
        const bool last = (s + 1 == NSEG);
        const unsigned pb = (unsigned)(((s & 1) * (NA * 80) + tid) * 16);
        const unsigned zb = (unsigned)((((s & 1) * NC + c) * (NB * 32) + tid) * 16);
        const unsigned tag = (unsigned)(s + 1);
        v4u r0 = (v4u){0u, last ? tag : 0u, 0u, last ? tag : 0u}, r1 = r0, r2 = r0, r3 = r0, r4 = r0;
        v4u y0 = (v4u){0u, 0u, 0u, 0u}, y1 = y0;
        unsigned spins = 0;
        for (;;) {
            if (!TAGOK(y0, tag)) y0 = ld_sc1(zrs, zb);
            if (!TAGOK(y1, tag)) y1 = ld_sc1(zrs, zb + 512 * 16);
            if (!TAGOK(r0, tag)) r0 = ld_sc1(prs, pb);
            if (!TAGOK(r1, tag)) r1 = ld_sc1(prs, pb + 512 * 16);
            if (!TAGOK(r2, tag)) r2 = ld_sc1(prs, pb + 1024 * 16);
            if (!TAGOK(r3, tag)) r3 = ld_sc1(prs, pb + 1536 * 16);
            if (!TAGOK(r4, tag)) r4 = ld_sc1(prs, pb + 2048 * 16);
            asm volatile("" ::: "memory");
            bool ok = TAGOK(r0, tag) & TAGOK(r1, tag) & TAGOK(r2, tag) & TAGOK(r3, tag) & TAGOK(r4, tag) & TAGOK(y0, tag) & TAGOK(y1, tag);
            if (__all(ok)) break;
            if (++spins > SPIN_CAP) { if (lane == 0) set_abort(a.ctl); return; }
            if ((spins & 1023u) == 0u && ld_ctl(a.ctl + CW(C_ABORT)) != 0u) return;
        }
        asm volatile("s_waitcnt vmcnt(0)" ::: "memory");
        STAMP(0);
        auto put8 = [&](u16* dst, v4u r) { v2u o; o.x = r.x; o.y = r.z; *reinterpret_cast<v2u*>(dst) = o; };
        if (!last) {
            put8(pimg + pdst[0], r0); put8(pimg + pdst[1], r1); put8(pimg + pdst[2], r2); put8(pimg + pdst[3], r3); put8(pimg + pdst[4], r4);
        }
        put8(zimg + zdst[0], y0); put8(zimg + zdst[1], y1);
        __syncthreads();
        STAMP(1);
        if (!last) {
            f32x4 acc = (f32x4){0.f, 0.f, 0.f, 0.f};
            const u16* prow = pimg + (lane & 7) * C_PLD + fq * 8;
#pragma unroll
            for (int k = 0; k < 4; ++k) {
                bf16x8 pf = *reinterpret_cast<const bf16x8*>(prow + wave * 128 + k * 32);
                acc = MFMA(uf[k], pf, acc);
            }
            {
                bf16x8 pf = *reinterpret_cast<const bf16x8*>(prow + 1024 + wave * 32);
                bf16x8 zf = *reinterpret_cast<const bf16x8*>(zimg + fr * Z_LD + wave * 32 + fq * 8);
                acc = MFMA(zf, pf, acc);
            }
            *reinterpret_cast<f32x4*>(red + (wave * 64 + lane) * 4) = acc;
        }
        v4u zo = *reinterpret_cast<const v4u*>(zimg + (tid >> 5) * Z_LD + (tid & 31) * 8);
        __syncthreads();
        if (!last && wave == 0 && fr < 8) {
            f32x4 t = *reinterpret_cast<const f32x4*>(red + lane * 4);
#pragma unroll
            for (int w = 1; w < 8; ++w) {
                f32x4 u = *reinterpret_cast<const f32x4*>(red + (w * 64 + lane) * 4);
                t[0] += u[0]; t[1] += u[1]; t[2] += u[2]; t[3] += u[3];
            }
            v4u o; o.x = pack2(t[0], t[1]); o.y = tag; o.z = pack2(t[2], t[3]); o.w = tag;
            st_sc1_x4(a.memg + ((size_t)((s + 1) & 1) * NC + c) * 32 + fr * 4 + fq, o);
        }
        STAMP(2);
        if (s > 0 && tid == 64) __hip_atomic_fetch_add(a.ctl + CW(C_CH + s - 1), 1u, __ATOMIC_RELAXED, __HIP_MEMORY_SCOPE_AGENT);
        st_sc1_x4(a.ZT + ((long)s * DM + c * 16 + (tid >> 5)) * 256 + (tid & 31) * 8, zo);
        if (s + 2 < NSEG) { if (!u_load(s + 1)) return; }
    }
    wg_signal(a.ctl, C_CH + NSEG - 1);
}

__device__ __forceinline__ bool wg_wait(const Mega& a, char* smem, int w0, unsigned t0, int w1, unsigned t1, int w2, unsigned t2) {
    volatile int* bc = reinterpret_cast<volatile int*>(smem + L_BCAST);
    if (threadIdx.x < 64) {
        bool ok = wave_wait(a.ctl, w0, t0);
        if (ok && w1 >= 0) ok = wave_wait(a.ctl, w1, t1);
        if (ok && w2 >= 0) ok = wave_wait(a.ctl, w2, t2);
        __builtin_amdgcn_fence(__ATOMIC_ACQUIRE, "agent");
        asm volatile("s_waitcnt vmcnt(0)" ::: "memory");
        if (threadIdx.x == 0) bc[1] = ok ? 1 : 0;
    }
    __syncthreads();
    const bool r = bc[1] != 0;
    __syncthreads();
    return r;
}

template <int BM, int BN, int WM, int WN>
__device__ __forceinline__ void gemm_core(const u16* __restrict__ A, long lda, const u16* __restrict__ B, long ldb, int K, char* sm,
                                          f32x4 (&acc)[WM / 16][WN / 16], const int tid) {
    constexpr int ROWS = BM + BN, TB = ROWS * 128, NWN = BN / WN, L = ROWS / 64;
    static_assert(ROWS % 64 == 0, "rows");
    const int wave = tid >> 6, lane = tid & 63, fr = lane & 15, fq = lane >> 4;
    const int wr = wave / NWN, wc = wave % NWN;
    auto stage = [&](int kt, int buf) {
#pragma unroll
        for (int i = 0; i < L; ++i) {
            const int b = tid * 16 + i * 8192, r = b >> 7, c = ((b >> 4) & 7) ^ (r & 7);
            const u16* src = (r < BM) ? A + (long)r * lda + kt * 64 + c * 8 : B + (long)(r - BM) * ldb + kt * 64 + c * 8;
            __builtin_amdgcn_global_load_lds((const unsigned*)src, (unsigned*)(sm + buf * TB + b), 16, 0, 0);
        }
    };
#pragma unroll
    for (int m = 0; m < WM / 16; ++m)
#pragma unroll
        for (int n = 0; n < WN / 16; ++n) acc[m][n] = (f32x4){0.f, 0.f, 0.f, 0.f};
    const int nt = K >> 6;
    stage(0, 0);
    stage(1, 1);
    int buf = 0;
    const int sw = fr & 7;
    for (int t = 0; t < nt; ++t) {
        if (t + 1 < nt) {
            if constexpr (L == 6) asm volatile("s_waitcnt vmcnt(6)" ::: "memory");
            else if constexpr (L == 5) asm volatile("s_waitcnt vmcnt(5)" ::: "memory");
            else if constexpr (L == 3) asm volatile("s_waitcnt vmcnt(3)" ::: "memory");
            else asm volatile("s_waitcnt vmcnt(0)" ::: "memory");
        } else {
            asm volatile("s_waitcnt vmcnt(0)" ::: "memory");
        }
        __builtin_amdgcn_s_barrier();
        if (t + 2 < nt) stage(t + 2, buf == 0 ? 2 : buf - 1);
        const char* sa = sm + buf * TB + (wr * WM + fr) * 128;
        const char* sb = sm + buf * TB + (BM + wc * WN + fr) * 128;
#pragma unroll
        for (int kk = 0; kk < 2; ++kk) {
            const int co = ((kk * 4 + fq) ^ sw) * 16;
            bf16x8 af[WM / 16], bfg[WN / 16];
#pragma unroll
            for (int m = 0; m < WM / 16; ++m) af[m] = *reinterpret_cast<const bf16x8*>(sa + m * 2048 + co);
#pragma unroll
            for (int n = 0; n < WN / 16; ++n) bfg[n] = *reinterpret_cast<const bf16x8*>(sb + n * 2048 + co);
#pragma unroll
            for (int m = 0; m < WM / 16; ++m)
#pragma unroll
                for (int n = 0; n < WN / 16; ++n) acc[m][n] = MFMA(bfg[n], af[m], acc[m][n]);
        }
        buf = (buf == 2) ? 0 : buf + 1;
    }
    asm volatile("s_waitcnt lgkmcnt(0)" ::: "memory");
    __syncthreads();
}

__device__ __forceinline__ void task_qk(const Mega& a, char* smem, int rb, int col0, const int tid) {
    f32x4 acc[4][4];
    gemm_core<128, 256, 64, 64>(a.xn + (long)rb * 128 * DM, DM, a.WT + (long)col0 * DM, DM, DM, smem, acc, tid);
    const int wave = tid >> 6, lane = tid & 63, fr = lane & 15, fq = lane >> 4, wr = wave >> 2, wc = wave & 3;
    u16* st = reinterpret_cast<u16*>(smem);
#pragma unroll
    for (int m = 0; m < 4; ++m)
#pragma unroll
        for (int n = 0; n < 4; ++n) {
            v2u o; o.x = pack2(acc[m][n][0], acc[m][n][1]); o.y = pack2(acc[m][n][2], acc[m][n][3]);
            *reinterpret_cast<v2u*>(st + (wr * 64 + m * 16 + fr) * 264 + wc * 64 + n * 16 + fq * 4) = o;
        }
    __syncthreads();
#pragma unroll
    for (int i = 0; i < 8; ++i) {
        const int id = tid + 512 * i, row = id >> 5, c16 = id & 31;
        v4u v = *reinterpret_cast<const v4u*>(st + row * 264 + c16 * 8);
        st_sc1_x4(a.QK + (long)(rb * 128 + row) * QLD + col0 + c16 * 8, v);
    }
}

#define VU_V 0
#define VU_U (52 * 1024)
__device__ __forceinline__ void task_vu(const Mega& a, char* smem, int rb, int h, const int tid) {
    const int wave = tid >> 6, lane = tid & 63, fr = lane & 15, fq = lane >> 4;
    u16* vimg = reinterpret_cast<u16*>(smem + VU_V);
    u16* ust = reinterpret_cast<u16*>(smem + VU_U);
    {
        f32x4 acc[4][3];
        gemm_core<128, 192, 64, 48>(a.xn + (long)rb * 128 * DM, DM, a.WT + (long)(2 * HD + h * DH) * DM, DM, DM, smem, acc, tid);
        const int wr = wave >> 2, wc = wave & 3;
#pragma unroll
        for (int m = 0; m < 4; ++m)
#pragma unroll
            for (int n = 0; n < 3; ++n) {
                v2u o; o.x = pack2(acc[m][n][0], acc[m][n][1]); o.y = pack2(acc[m][n][2], acc[m][n][3]);
                *reinterpret_cast<v2u*>(vimg + (wr * 64 + m * 16 + fr) * 200 + wc * 48 + n * 16 + fq * 4) = o;
            }
    }
    bf16x8 wof[5][6];
#pragma unroll
    for (int ct = 0; ct < 5; ++ct)
#pragma unroll
        for (int ks = 0; ks < 6; ++ks)
            wof[ct][ks] = ld_frag(a.WoT + ((long)h * DM + (wave * 5 + ct) * 16 + fr) * DH + ks * 32 + fq * 8);
    __syncthreads();
    for (int sg = 0; sg < 4; ++sg) {
        f32x4 acc[5][2];
#pragma unroll
        for (int ct = 0; ct < 5; ++ct) { acc[ct][0] = (f32x4){0.f, 0.f, 0.f, 0.f}; acc[ct][1] = acc[ct][0]; }
        const u16* v0 = vimg + (sg * 32 + fr) * 200 + fq * 8;
#pragma unroll
        for (int ks = 0; ks < 6; ++ks) {
            bf16x8 a0 = *reinterpret_cast<const bf16x8*>(v0 + ks * 32);
            bf16x8 a1 = *reinterpret_cast<const bf16x8*>(v0 + 16 * 200 + ks * 32);
#pragma unroll
            for (int ct = 0; ct < 5; ++ct) { acc[ct][0] = MFMA(a0, wof[ct][ks], acc[ct][0]); acc[ct][1] = MFMA(a1, wof[ct][ks], acc[ct][1]); }
        }
#pragma unroll
        for (int ct = 0; ct < 5; ++ct)
#pragma unroll
            for (int rt = 0; rt < 2; ++rt) {
                v2u o; o.x = pack2(acc[ct][rt][0], acc[ct][rt][1]); o.y = pack2(acc[ct][rt][2], acc[ct][rt][3]);
                *reinterpret_cast<v2u*>(ust + ((wave * 5 + ct) * 16 + fr) * 32 + rt * 16 + fq * 4) = o;
            }
        __syncthreads();
        u16* dst = a.UT + ((long)(rb * 4 + sg) * NH + h) * (DM * 32);
#pragma unroll
        for (int i = 0; i < 5; ++i) {
            const int id = tid + 512 * i;
            v4u v = *reinterpret_cast<const v4u*>(ust + id * 8);
            st_sc1_x4(dst + id * 8, v);
        }
        __syncthreads();
    }
}

__device__ __forceinline__ void task_p(const Mega& a, int seg, int hg, const int tid) {
    const int wave = tid >> 6, lane = tid & 63, fr = lane & 15, fq = lane >> 4;
    const float scale = 0.07216878364870322f;
    const int h = hg * 8 + wave;
    const u16* kmp = a.km + (((long)seg * NH + h) * 8 + (fr & 7)) * DH + fq * 8;
    const u16* kx0 = a.QK + (long)(seg * 32 + fr) * QLD + HD + h * DH + fq * 8;
    const u16* qp = a.QK + (long)(seg * 32 + fr) * QLD + h * DH + fq * 8;
    bf16x8 kmf[6], kf0[6], kf1[6], qf[2][6];
#pragma unroll
    for (int ks = 0; ks < 6; ++ks) {
        qf[0][ks] = ld_frag(qp + ks * 32); kmf[ks] = ld_frag(kmp + ks * 32); kf0[ks] = ld_frag(kx0 + ks * 32);
        kf1[ks] = ld_frag(kx0 + (long)16 * QLD + ks * 32); qf[1][ks] = ld_frag(qp + (long)16 * QLD + ks * 32);
    }
#pragma unroll
    for (int qt = 0; qt < 2; ++qt) {
        f32x4 s0 = (f32x4){0.f, 0.f, 0.f, 0.f}, s1 = s0, s2 = s0;
#pragma unroll
        for (int ks = 0; ks < 6; ++ks) {
            s0 = MFMA(kmf[ks], qf[qt][ks], s0);
            s1 = MFMA(kf0[ks], qf[qt][ks], s1);
            if (qt == 1) s2 = MFMA(kf1[ks], qf[qt][ks], s2);
        }
        const int ql = qt * 16 + fr;
        float v[12]; float mx = -3.0e38f;
#pragma unroll
        for (int r = 0; r < 4; ++r) {
            v[r] = (fq < 2) ? bfr(bfr(s0[r]) * scale) : -1.0e9f;
            v[4 + r] = (fq * 4 + r <= ql) ? bfr(bfr(s1[r]) * scale) : -1.0e9f;
            v[8 + r] = (16 + fq * 4 + r <= ql) ? bfr(bfr(s2[r]) * scale) : -1.0e9f;
        }
#pragma unroll
        for (int j = 0; j < 12; ++j) mx = fmaxf(mx, v[j]);
        mx = max4g(mx);
        float sum = 0.f;
#pragma unroll
        for (int j = 0; j < 12; ++j) { v[j] = (j < 4 && fq >= 2) ? 0.f : __expf(v[j] - mx); sum += v[j]; }
        sum = sum4g(sum);
        const float inv = 1.0f / sum;
        u16* prow = a.Pbuf + ((long)seg * 32 + ql) * 1280;
        if (fq < 2) {
            v2u o; o.x = pack2(v[0] * inv, v[1] * inv); o.y = pack2(v[2] * inv, v[3] * inv);
            st_sc1_x2(prow + 1024 + h * 8 + fq * 4, o);
        }
        v2u o; o.x = pack2(v[4] * inv, v[5] * inv); o.y = pack2(v[6] * inv, v[7] * inv);
        st_sc1_x2(prow + h * 32 + fq * 4, o);
        o.x = pack2(v[8] * inv, v[9] * inv); o.y = pack2(v[10] * inv, v[11] * inv);
        st_sc1_x2(prow + h * 32 + 16 + fq * 4, o);
    }
}

#define XP_LD 1288
__device__ __forceinline__ void task_o(const Mega& a, char* smem, int seg, int cb, const int tid) {
    u16* P = reinterpret_cast<u16*>(smem);
    const int wave = tid >> 6, lane = tid & 63, fr = lane & 15, fq = lane >> 4;
#pragma unroll
    for (int i = 0; i < 10; ++i) {
        const int id = tid + 512 * i, row = id / 160, c = (id % 160) * 8;
        *reinterpret_cast<v4u*>(P + row * XP_LD + c) = *reinterpret_cast<const v4u*>(a.Pbuf + ((long)seg * 32 + row) * 1280 + c);
    }
    const int col0 = cb * 128 + wave * 16;
    f32x4 a0 = (f32x4){0.f, 0.f, 0.f, 0.f}, a1 = a0;
    const u16* up = a.UT + ((long)seg * NH * DM + col0 + fr) * 32 + fq * 8;
    const u16* zp = a.ZT + ((long)seg * DM + col0 + fr) * 256 + fq * 8;
    bf16x8 uf[8];
#pragma unroll
    for (int k = 0; k < 8; ++k) uf[k] = ld_frag(up + (long)k * (DM * 32));
    __syncthreads();
    const u16* p0 = P + fr * XP_LD + fq * 8;
    const u16* p1 = P + (16 + fr) * XP_LD + fq * 8;
#pragma unroll
    for (int kb = 0; kb < 5; ++kb) {
        bf16x8 nf[8];
#pragma unroll
        for (int k = 0; k < 8; ++k) {
            if (kb < 3) nf[k] = ld_frag(up + (long)((kb + 1) * 8 + k) * (DM * 32));
            else if (kb == 3) nf[k] = ld_frag(zp + k * 32);
        }
#pragma unroll
        for (int k = 0; k < 8; ++k) {
            const int ks = kb * 8 + k;
            a0 = MFMA(uf[k], *reinterpret_cast<const bf16x8*>(p0 + ks * 32), a0);
            a1 = MFMA(uf[k], *reinterpret_cast<const bf16x8*>(p1 + ks * 32), a1);
        }
        if (kb < 4) {
#pragma unroll
            for (int k = 0; k < 8; ++k) uf[k] = nf[k];
        }
    }
#pragma unroll
    for (int qt = 0; qt < 2; ++qt) {
        const f32x4 av = qt ? a1 : a0;
        const long off = (long)(seg * 32 + qt * 16 + fr) * DM + col0 + fq * 4;
        v2u xi = *reinterpret_cast<const v2u*>(a.x + off);
        v2u o;
        o.x = pack2(bf2f((u16)(xi.x & 0xffff)) + bfr(av[0]), bf2f((u16)(xi.x >> 16)) + bfr(av[1]));
        o.y = pack2(bf2f((u16)(xi.y & 0xffff)) + bfr(av[2]), bf2f((u16)(xi.y >> 16)) + bfr(av[3]));
        st_sc1_x2(a.hbuf + off, o);
    }
}

__device__ __forceinline__ float gelu_tanh(float x) {
    const float u = 0.7978845608028654f * (x + 0.044715f * x * x * x);
    return __fdividef(x, 1.0f + __expf(-2.0f * u));
}
#define M1_A 0
#define M1_B (82 * 1024)
#define M1_ST 0
__device__ __forceinline__ void task_mlp1(const Mega& a, char* smem, int b64, int ct, const int tid) {
    const int wave = tid >> 6, lane = tid & 63, fr = lane & 15, fq = lane >> 4, wr = wave >> 2, wc = wave & 3;
    u16* aimg = reinterpret_cast<u16*>(smem + M1_A);
    const u16* Bg = a.w1T + (long)ct * 128 * DM;
    auto stage = [&](int kt) {
        const int b = tid * 16, r = b >> 6, c = ((b >> 4) & 3) ^ ((r >> 2) & 3);
        __builtin_amdgcn_global_load_lds((const unsigned*)(Bg + (long)r * DM + kt * 32 + c * 8), (unsigned*)(smem + M1_B + (kt & 7) * 8192 + b), 16, 0, 0);
    };
#pragma unroll
    for (int i = 0; i < 7; ++i) stage(i);
#pragma unroll
    for (int i = 0; i < 10; ++i) {
        const int id = tid + 512 * i, row = id / 80, c = (id % 80) * 8;
        *reinterpret_cast<v4u*>(aimg + row * MEM_LD + c) = *reinterpret_cast<const v4u*>(a.hbuf + (long)(b64 * 64 + row) * DM + c);
    }
    __syncthreads();
#pragma unroll
    for (int ps = 0; ps < 2; ++ps) {
        u16* rp = aimg + (wave * 8 + ps * 4 + fq) * MEM_LD + fr * 8;
        float v[40];
#pragma unroll
        for (int j = 0; j < 5; ++j) {
            v4u t = *reinterpret_cast<const v4u*>(rp + j * 128);
            v[j * 8 + 0] = __uint_as_float(t.x << 16); v[j * 8 + 1] = __uint_as_float(t.x & 0xffff0000u);
            v[j * 8 + 2] = __uint_as_float(t.y << 16); v[j * 8 + 3] = __uint_as_float(t.y & 0xffff0000u);
            v[j * 8 + 4] = __uint_as_float(t.z << 16); v[j * 8 + 5] = __uint_as_float(t.z & 0xffff0000u);
            v[j * 8 + 6] = __uint_as_float(t.w << 16); v[j * 8 + 7] = __uint_as_float(t.w & 0xffff0000u);
        }
#pragma unroll
        for (int rep = 0; rep < 2; ++rep) {
            float sm = 0.f;
#pragma unroll
            for (int i = 0; i < 40; ++i) sm += v[i];
            sm += __shfl_xor(sm, 1); sm += __shfl_xor(sm, 2); sm += __shfl_xor(sm, 4); sm += __shfl_xor(sm, 8);
            const float mean = sm * (1.0f / DM);
            float q = 0.f;
#pragma unroll
            for (int i = 0; i < 40; ++i) { const float d = v[i] - mean; q += d * d; }
            q += __shfl_xor(q, 1); q += __shfl_xor(q, 2); q += __shfl_xor(q, 4); q += __shfl_xor(q, 8);
            const float mb = bfr(mean);
            const float rden = 1.0f / bfr(sqrtf(bfr(bfr(q * (1.0f / DM)) + 1e-5f)));
#pragma unroll
            for (int i = 0; i < 40; ++i) v[i] = bfr(bfr(v[i] - mb) * rden);
        }
#pragma unroll
        for (int j = 0; j < 5; ++j) {
            v4u t;
            t.x = pack2(v[j * 8 + 0], v[j * 8 + 1]); t.y = pack2(v[j * 8 + 2], v[j * 8 + 3]);
            t.z = pack2(v[j * 8 + 4], v[j * 8 + 5]); t.w = pack2(v[j * 8 + 6], v[j * 8 + 7]);
            *reinterpret_cast<v4u*>(rp + j * 128) = t;
        }
    }
    f32x4 acc[2][2];
#pragma unroll
    for (int m = 0; m < 2; ++m)
#pragma unroll
        for (int n = 0; n < 2; ++n) acc[m][n] = (f32x4){0.f, 0.f, 0.f, 0.f};
    const int sw = (fq ^ ((fr >> 2) & 3)) * 16;
    for (int t = 0; t < 20; ++t) {
        if (t + 6 < 20) asm volatile("s_waitcnt vmcnt(6) lgkmcnt(0)" ::: "memory");
        else asm volatile("s_waitcnt vmcnt(0) lgkmcnt(0)" ::: "memory");
        __builtin_amdgcn_s_barrier();
        if (t + 7 < 20) stage(t + 7);
        bf16x8 af[2], bfg[2];
#pragma unroll
        for (int m = 0; m < 2; ++m) af[m] = *reinterpret_cast<const bf16x8*>(aimg + (wr * 32 + m * 16 + fr) * MEM_LD + t * 32 + fq * 8);
#pragma unroll
        for (int n = 0; n < 2; ++n) bfg[n] = *reinterpret_cast<const bf16x8*>(smem + M1_B + (t & 7) * 8192 + (wc * 32 + n * 16 + fr) * 64 + sw);
#pragma unroll
        for (int m = 0; m < 2; ++m)
#pragma unroll
            for (int n = 0; n < 2; ++n) acc[m][n] = MFMA(bfg[n], af[m], acc[m][n]);
    }
    asm volatile("s_waitcnt lgkmcnt(0)" ::: "memory");
    __syncthreads();
    u16* st = reinterpret_cast<u16*>(smem + M1_ST);
#pragma unroll
    for (int n = 0; n < 2; ++n) {
        const int cl = wc * 32 + n * 16 + fq * 4;
        v2u bb = *reinterpret_cast<const v2u*>(a.b1 + ct * 128 + cl);
        const float b0 = bf2f((u16)(bb.x & 0xffff)), b1 = bf2f((u16)(bb.x >> 16)), b2 = bf2f((u16)(bb.y & 0xffff)), b3 = bf2f((u16)(bb.y >> 16));
#pragma unroll
        for (int m = 0; m < 2; ++m) {
            v2u o;
            o.x = pack2(gelu_tanh(bfr(bfr(acc[m][n][0]) + b0)), gelu_tanh(bfr(bfr(acc[m][n][1]) + b1)));
            o.y = pack2(gelu_tanh(bfr(bfr(acc[m][n][2]) + b2)), gelu_tanh(bfr(bfr(acc[m][n][3]) + b3)));
            *reinterpret_cast<v2u*>(st + (wr * 32 + m * 16 + fr) * 136 + cl) = o;
        }
    }
    __syncthreads();
#pragma unroll
    for (int i = 0; i < 2; ++i) {
        const int id = tid + 512 * i, row = id >> 4, c16 = id & 15;
        v4u v = *reinterpret_cast<const v4u*>(st + row * 136 + c16 * 8);
        st_sc1_x4(a.hid + (long)(b64 * 64 + row) * FF + ct * 128 + c16 * 8, v);
    }
}

__device__ __forceinline__ void task_mlp2(const Mega& a, char* smem, int b64, int ct, const int tid) {
    f32x4 acc[2][2];
    gemm_core<64, 128, 32, 32>(a.hid + (long)b64 * 64 * FF, FF, a.w2T + (long)ct * 128 * FF, FF, FF, smem, acc, tid);
    const int wave = tid >> 6, lane = tid & 63, fr = lane & 15, fq = lane >> 4, wr = wave >> 2, wc = wave & 3;
#pragma unroll
    for (int n = 0; n < 2; ++n) {
        const int col = ct * 128 + wc * 32 + n * 16 + fq * 4;
        v2u bb = *reinterpret_cast<const v2u*>(a.b2 + col);
        const float b0 = bf2f((u16)(bb.x & 0xffff)), b1 = bf2f((u16)(bb.x >> 16)), b2 = bf2f((u16)(bb.y & 0xffff)), b3 = bf2f((u16)(bb.y >> 16));
#pragma unroll
        for (int m = 0; m < 2; ++m) {
            const long off = (long)(b64 * 64 + wr * 32 + m * 16 + fr) * DM + col;
            v2u hi = *reinterpret_cast<const v2u*>(a.hbuf + off);
            v2u o;
            o.x = pack2(bf2f((u16)(hi.x & 0xffff)) + bfr(bfr(acc[m][n][0]) + b0), bf2f((u16)(hi.x >> 16)) + bfr(bfr(acc[m][n][1]) + b1));
            o.y = pack2(bf2f((u16)(hi.y & 0xffff)) + bfr(bfr(acc[m][n][2]) + b2), bf2f((u16)(hi.y >> 16)) + bfr(bfr(acc[m][n][3]) + b3));
            *reinterpret_cast<v2u*>(a.out + off) = o;
        }
    }
}

#define T_P1 896
#define T_P2 (T_P1 + 384)
#define T_END (T_P2 + 32 * 35)
__device__ __forceinline__ void worker(const Mega& a, char* smem) {
    volatile int* bc = reinterpret_cast<volatile int*>(smem + L_BCAST);
    for (;;) {
        if (threadIdx.x == 0) bc[0] = (int)__hip_atomic_fetch_add(a.ctl + CW(C_TICKET), 1u, __ATOMIC_RELAXED, __HIP_MEMORY_SCOPE_AGENT);
        __syncthreads();
        const int t = __builtin_amdgcn_readfirstlane(bc[0]);
        __syncthreads();
        if (t >= T_END) return;
        int tid = threadIdx.x;
        asm volatile("" : "+v"(tid));
        if (t < T_P1) {
            const int rb = t / 56, r = t % 56;
            if (r < 32) { task_vu(a, smem, rb, r, tid); wg_signal(a.ctl, C_U + rb); }
            else { task_qk(a, smem, rb, HD + (r - 32) * 256, tid); wg_signal(a.ctl, C_K + rb); }
        } else if (t < T_P2) {
            const int u = t - T_P1, rb = u / 24, ct = u % 24;
            task_qk(a, smem, rb, ct * 256, tid); wg_signal(a.ctl, C_Q + rb);
        } else {
            const int u = t - T_P2, b = u / 35, r = u % 35;
            if (r < 8) {
                const int seg = 2 * b + (r >> 2);
                if (!wg_wait(a, smem, C_Q + (seg >> 2), 24u, C_CH + seg, (unsigned)(NA + NC), -1, 0u)) return;
                task_p(a, seg, r & 3, tid); wg_signal(a.ctl, C_P + seg);
            } else if (r < 18) {
                const int seg = 2 * b + (r - 8) / 5;
                if (!wg_wait(a, smem, C_P + seg, 4u, -1, 0u, -1, 0u)) return;
                task_o(a, smem, seg, (r - 8) % 5, tid); wg_signal(a.ctl, C_X + b);
            } else if (r < 30) {
                if (!wg_wait(a, smem, C_X + b, 10u, -1, 0u, -1, 0u)) return;
                task_mlp1(a, smem, b, r - 18, tid); wg_signal(a.ctl, C_M + b);
            } else {
                if (!wg_wait(a, smem, C_M + b, 12u, -1, 0u, -1, 0u)) return;
                task_mlp2(a, smem, b, r - 30, tid);
            }
        }
        __syncthreads();
    }
}

__global__ __launch_bounds__(512, 2) void k_mega(Mega a) {
    __shared__ __attribute__((aligned(16))) char smem[160 * 1024];
    const int b = blockIdx.x;
    if (b < NA) {
        role_a(a, smem, b);
    } else if (b < NA + NB) {
        const int h = b - NA;
        const int wave = __builtin_amdgcn_readfirstlane(threadIdx.x >> 6), lane = threadIdx.x & 63;
        if (wave < 4) role_b_wave<2, 4, 21>(a, smem, h, lane, 2 * wave, 4 * wave, wave * 21 * 1024);
        else role_b_wave<1, 6, 13>(a, smem, h, lane, 8 + (wave - 4), 16 + 6 * (wave - 4), (84 + (wave - 4) * 13) * 1024);
    } else if (b < NCHAIN) {
        role_c(a, smem, b - NA - NB);
    } else {
        worker(a, smem);
    }
}

extern "C" void kernel_launch(void* const* d_in, const int* in_sizes, int n_in, void* d_out, int out_size, void* d_ws,
                              size_t ws_size, hipStream_t stream) {
    char* ws = (char*)d_ws;
    size_t off = 0;
    auto take = [&](size_t bytes) { char* p = ws + off; off += (bytes + 255) & ~(size_t)255; return p; };
    const size_t gran_bytes = (size_t)(2 * NC * 32 + 2 * NA * 80 + 2 * NC * NB * 32) * 16;
    const size_t comm_bytes = gran_bytes + (size_t)C_NWORDS * 64;
    char* comm = take(comm_bytes);
    u16* xn = (u16*)take((size_t)NTOK * DM * 2);
    u16* WT = (u16*)take((size_t)3 * HD * DM * 2);
    u16* WoT = (u16*)take((size_t)NH * DM * DH * 2);
    u16* w1T = (u16*)take((size_t)FF * DM * 2);
    u16* w2T = (u16*)take((size_t)DM * FF * 2);
    u16* QK = (u16*)take((size_t)NTOK * QLD * 2);
    u16* UT = (u16*)take((size_t)NSEG * NH * DM * 32 * 2);
    u16* km = (u16*)take((size_t)NSEG * NH * 8 * DH * 2);
    u16* ZT = (u16*)take((size_t)NSEG * DM * 256 * 2);
    u16* hbuf = (u16*)take((size_t)NTOK * DM * 2);
    u16* hid = (u16*)take((size_t)NTOK * FF * 2);
    u16* Pbuf = (u16*)take((size_t)NTOK * 1280 * 2);

    (void)hipMemsetAsync(comm, 0, comm_bytes, stream);
    PreArgs p;
    p.Wq = (const u16*)d_in[2]; p.Wk = (const u16*)d_in[3]; p.Wv = (const u16*)d_in[4]; p.Wo = (const u16*)d_in[5];
    p.w1 = (const u16*)d_in[8]; p.w2 = (const u16*)d_in[10]; p.x = (const u16*)d_in[0];
    p.WT = WT; p.WoT = WoT; p.w1T = w1T; p.w2T = w2T; p.xn = xn;
    k_pre<<<PRE_BLOCKS, 256, 0, stream>>>(p);

    Mega a;
    a.x = (const u16*)d_in[0]; a.mem0 = (const u16*)d_in[1]; a.b1 = (const u16*)d_in[9]; a.b2 = (const u16*)d_in[11];
    a.xn = xn; a.WT = WT; a.WoT = WoT; a.w1T = w1T; a.w2T = w2T;
    a.QK = QK; a.UT = UT; a.km = km; a.ZT = ZT; a.hbuf = hbuf; a.hid = hid; a.out = (u16*)d_out; a.Pbuf = Pbuf;
    a.memg = (v4u*)comm; a.pg = a.memg + 2 * NC * 32; a.zg = a.pg + 2 * NA * 80;
    a.ctl = (unsigned*)(comm + gran_bytes);
    k_mega<<<256, 512, 0, stream>>>(a);
}
```

```cpp
#include <hip/hip_runtime.h>
#include <hip/hip_bf16.h>

typedef unsigned short u16;
typedef unsigned long long u64;
using bf16x8 = __attribute__((ext_vector_type(8))) short;
using f32x4 = __attribute__((ext_vector_type(4))) float;
typedef unsigned v4u __attribute__((ext_vector_type(4)));
typedef unsigned v2u __attribute__((ext_vector_type(2)));

#define NSEG 64
#define DM 640
#define NH 32
#define DH 192
#define HD 6144
#define QLD 12288
#define NTOK 2048
#define FF 1536

__device__ __forceinline__ float bf2f(u16 v) { return __uint_as_float(((unsigned)v) << 16); }
__device__ __forceinline__ u16 f2bf(float f) {
    __hip_bfloat16 h = __float2bfloat16(f);
    return *reinterpret_cast<u16*>(&h);
}
__device__ __forceinline__ float bfr(float f) { return bf2f(f2bf(f)); }
__device__ __forceinline__ unsigned pack2(float a, float b) { return (unsigned)f2bf(a) | ((unsigned)f2bf(b) << 16); }
__device__ __forceinline__ bf16x8 ld_frag(const u16* p) { return *reinterpret_cast<const bf16x8*>(p); }
#define MFMA(a, b, c) __builtin_amdgcn_mfma_f32_16x16x32_bf16((a), (b), (c), 0, 0, 0)
__device__ __forceinline__ float max4g(float x) {
    auto r = __builtin_amdgcn_permlane16_swap(__float_as_uint(x), __float_as_uint(x), false, false);
    x = fmaxf(__uint_as_float(r[0]), __uint_as_float(r[1]));
    auto q = __builtin_amdgcn_permlane32_swap(__float_as_uint(x), __float_as_uint(x), false, false);
    return fmaxf(__uint_as_float(q[0]), __uint_as_float(q[1]));
}
__device__ __forceinline__ float sum4g(float x) {
    auto r = __builtin_amdgcn_permlane16_swap(__float_as_uint(x), __float_as_uint(x), false, false);
    x = __uint_as_float(r[0]) + __uint_as_float(r[1]);
    auto q = __builtin_amdgcn_permlane32_swap(__float_as_uint(x), __float_as_uint(x), false, false);
    return __uint_as_float(q[0]) + __uint_as_float(q[1]);
}

__device__ __forceinline__ float wave_sum(float v) {
#pragma unroll
    for (int m = 32; m >= 1; m >>= 1) v += __shfl_xor(v, m);
    return v;
}
__device__ __forceinline__ void ln_row(float (&v)[10]) {
    float s = 0.f;
#pragma unroll
    for (int i = 0; i < 10; ++i) s += v[i];
    s = wave_sum(s);
    float mean = s * (1.0f / DM);
    float q = 0.f;
#pragma unroll
    for (int i = 0; i < 10; ++i) { float d = v[i] - mean; q += d * d; }
    q = wave_sum(q);
    float mb = bfr(mean);
    float vb = bfr(q * (1.0f / DM));
    float den = bfr(sqrtf(bfr(vb + 1e-5f)));
#pragma unroll
    for (int i = 0; i < 10; ++i) v[i] = bfr(bfr(v[i] - mb) / den);
}

template <int R>
__device__ __forceinline__ void ln_rows(float (&v)[R][10]) {
    float s[R], q[R];
#pragma unroll
    for (int r = 0; r < R; ++r) {
        s[r] = 0.f;
#pragma unroll
        for (int i = 0; i < 10; ++i) s[r] += v[r][i];
    }
#pragma unroll
    for (int m = 32; m >= 1; m >>= 1)
#pragma unroll
        for (int r = 0; r < R; ++r) s[r] += __shfl_xor(s[r], m);
#pragma unroll
    for (int r = 0; r < R; ++r) {
        s[r] *= (1.0f / DM);
        q[r] = 0.f;
#pragma unroll
        for (int i = 0; i < 10; ++i) { float d = v[r][i] - s[r]; q[r] += d * d; }
    }
#pragma unroll
    for (int m = 32; m >= 1; m >>= 1)
#pragma unroll
        for (int r = 0; r < R; ++r) q[r] += __shfl_xor(q[r], m);
#pragma unroll
    for (int r = 0; r < R; ++r) {
        const float mb = bfr(s[r]);
        const float den = bfr(sqrtf(bfr(bfr(q[r] * (1.0f / DM)) + 1e-5f)));
        const float rden = 1.0f / den;
#pragma unroll
        for (int i = 0; i < 10; ++i) v[r][i] = bfr(bfr(v[r][i] - mb) * rden);
    }
}

struct PreArgs {
    const u16 *Wq, *Wk, *Wv, *Wo, *w1, *w2, *x;
    u16 *WT, *WoT, *w1T, *w2T, *xn;
    v4u* zero; long nzero16;
};
__device__ __forceinline__ void tr_tile(const u16* ib, u16* ob, int R, int C, int r0, int c0, u16 (*t)[72]) {
    const int tid = threadIdx.x;
#pragma unroll
    for (int i = 0; i < 2; ++i) {
        const int id = tid + 256 * i, r = id >> 3, c = (id & 7) * 8;
        const v4u v = *reinterpret_cast<const v4u*>(ib + (long)(r0 + r) * C + c0 + c);
        t[c + 0][r] = (u16)(v.x & 0xffff); t[c + 1][r] = (u16)(v.x >> 16);
        t[c + 2][r] = (u16)(v.y & 0xffff); t[c + 3][r] = (u16)(v.y >> 16);
        t[c + 4][r] = (u16)(v.z & 0xffff); t[c + 5][r] = (u16)(v.z >> 16);
        t[c + 6][r] = (u16)(v.w & 0xffff); t[c + 7][r] = (u16)(v.w >> 16);
    }
    __syncthreads();
#pragma unroll
    for (int i = 0; i < 2; ++i) {
        const int id = tid + 256 * i, c = id >> 3, r = (id & 7) * 8;
        *reinterpret_cast<v4u*>(ob + (long)(c0 + c) * R + r0 + r) = *reinterpret_cast<const v4u*>(&t[c][r]);
    }
}
__global__ __launch_bounds__(256) void k_pre(PreArgs p) {
    __shared__ __attribute__((aligned(16))) u16 t[64][72];
    int b = blockIdx.x;
    if (b < 512) {
        int row = b * 4 + (threadIdx.x >> 6), lane = threadIdx.x & 63;
        float v[10];
#pragma unroll
        for (int i = 0; i < 10; ++i) v[i] = bf2f(p.x[(long)row * DM + lane + 64 * i]);
        ln_row(v);
#pragma unroll
        for (int i = 0; i < 10; ++i) p.xn[(long)row * DM + lane + 64 * i] = f2bf(v[i]);
        return;
    }
    b -= 512;
    if (b < 2880) {
        int w = b / 960, r = b % 960;
        const u16* src = w == 0 ? p.Wq : (w == 1 ? p.Wk : p.Wv);
        tr_tile(src, p.WT + (size_t)w * HD * DM, DM, HD, (r % 10) * 64, (r / 10) * 64, t);
        return;
    }
    b -= 2880;
    if (b < 960) {
        int h = b / 30, r = b % 30;
        tr_tile(p.Wo + (size_t)h * DH * DM, p.WoT + (size_t)h * DM * DH, DH, DM, (r % 3) * 64, (r / 3) * 64, t);
        return;
    }
    b -= 960;
    if (b < 240) {
        tr_tile(p.w1, p.w1T, DM, FF, (b % 10) * 64, (b / 10) * 64, t);
        return;
    }
    b -= 240;
    if (b < 240) {
        tr_tile(p.w2, p.w2T, FF, DM, (b % 24) * 64, (b / 24) * 64, t);
        return;
    }
    b -= 240;
    for (long i = (long)b * 256 + threadIdx.x; i < p.nzero16; i += 128 * 256) p.zero[i] = (v4u){0u, 0u, 0u, 0u};
}
#define PRE_BLOCKS (512 + 2880 + 960 + 240 + 240 + 128)

#define NA 32
#define NB 32
#define NC 40
#define NCHAIN (NA + NB + 2 * NC)
#define SPIN_CAP 3000000u
#define GATE_A 40
#define GATE_B 64
#define GATE_C 64

struct Mega {
    const u16 *x, *mem0, *b1, *b2;
    const u16 *xn, *WT, *WoT, *w1T, *w2T;
    u16 *QK, *UT, *km, *ZT, *hbuf, *hid, *out, *Pbuf;
    v4u* memg;
    v4u* pg;
    v4u* zg;
    unsigned* ctl;
};
#define CW(i) ((i) * 16)
#define C_ABORT 0
#define C_TICKET 1
#define C_K 2
#define C_U 18
#define C_Q 34
#define C_CH 50
#define C_X 114
#define C_M 146
#define C_P 178
#define C_U0 242
#define C_NWORDS 243

#define STAMP(slot) do {} while (0)

#define MEM_LD 648
#define QK_LD 200
#define L_W 0
#define L_KX (128 * 1024)
#define L_MEM (140 * 1024)
#define L_QK (L_MEM + 10368)
#define L_END (L_QK + 6400)
static_assert(L_END <= 160 * 1024 - 64, "lds");
#define C_PLD 1288
#define Z_LD 264
#define LC_P 0
#define LC_Z (LC_P + 20608)
#define LC_RED (LC_Z + 8448)
#define L_BCAST (160 * 1024 - 64)

__device__ __forceinline__ void st_sc1_x4(void* p, v4u v) {
    asm volatile("global_store_dwordx4 %0, %1, off sc1\n\ts_nop 1" ::"v"(p), "v"(v) : "memory");
}
__device__ __forceinline__ void st_sc1_x2(void* p, v2u v) {
    asm volatile("global_store_dwordx2 %0, %1, off sc1\n\ts_nop 0" ::"v"(p), "v"(v) : "memory");
}
__device__ __forceinline__ v4u ld_sc1(__amdgpu_buffer_rsrc_t r, unsigned off) { return __builtin_amdgcn_raw_buffer_load_b128(r, off, 0, 16); }
#define TAGOK(r, tag) (((r).y == (tag)) & ((r).w == (tag)))
__device__ __forceinline__ unsigned ld_ctl(unsigned* p) {
    return (unsigned)__builtin_amdgcn_readfirstlane(__hip_atomic_load(p, __ATOMIC_RELAXED, __HIP_MEMORY_SCOPE_AGENT));
}
__device__ __forceinline__ void set_abort(unsigned* ctl) { __hip_atomic_store(ctl + CW(C_ABORT), 1u, __ATOMIC_RELAXED, __HIP_MEMORY_SCOPE_AGENT); }
__device__ __forceinline__ bool wave_wait(unsigned* ctl, int word, unsigned target) {
    unsigned spins = 0;
    while (ld_ctl(ctl + CW(word)) < target) {
        __builtin_amdgcn_s_sleep(4);
        if (++spins > SPIN_CAP) { set_abort(ctl); return false; }
        if ((spins & 255u) == 0u && ld_ctl(ctl + CW(C_ABORT)) != 0u) return false;
    }
    return true;
}
__device__ __forceinline__ void wg_signal(unsigned* ctl, int word) {
    asm volatile("s_waitcnt vmcnt(0)" ::: "memory");
    __syncthreads();
    if (threadIdx.x == 0) __hip_atomic_fetch_add(ctl + CW(word), 1u, __ATOMIC_RELAXED, __HIP_MEMORY_SCOPE_AGENT);
}

__device__ __forceinline__ bool gather_mem(const Mega& a, char* smem, int s, int tid) {
    u16* mimg = reinterpret_cast<u16*>(smem + L_MEM);
    if (s == 0) {
        for (int i = tid; i < 8 * 80; i += 512) {
            int m = i / 80, c = (i % 80) * 8;
            *reinterpret_cast<bf16x8*>(mimg + m * MEM_LD + c) = ld_frag(a.mem0 + m * DM + c);
        }
        asm volatile("s_waitcnt vmcnt(0)" ::: "memory");
        return true;
    }
    const __amdgpu_buffer_rsrc_t rs = __builtin_amdgcn_make_buffer_rsrc((void*)a.memg, 0, 2 * NC * 32 * 16, 0x00020000);
    const int i0 = tid, i1 = tid + 512, i2 = (tid < 256) ? tid + 1024 : tid;
    const unsigned ob = (unsigned)(s & 1) * (NC * 32 * 16);
    v4u r0 = (v4u){0u, 0u, 0u, 0u}, r1 = r0, r2 = r0;
    unsigned spins = 0;
    const unsigned tag = (unsigned)s;
    for (;;) {
        if (!TAGOK(r0, tag)) r0 = ld_sc1(rs, ob + i0 * 16);
        if (!TAGOK(r1, tag)) r1 = ld_sc1(rs, ob + i1 * 16);
        if (!TAGOK(r2, tag)) r2 = ld_sc1(rs, ob + i2 * 16);
        asm volatile("" ::: "memory");
        bool ok = TAGOK(r0, tag) & TAGOK(r1, tag) & TAGOK(r2, tag);
        if (__all(ok)) break;
        if (++spins > SPIN_CAP) { if ((tid & 63) == 0) set_abort(a.ctl); return false; }
        if ((spins & 1023u) == 0u && ld_ctl(a.ctl + CW(C_ABORT)) != 0u) return false;
    }
    asm volatile("s_waitcnt vmcnt(0)" ::: "memory");
    auto put = [&](int idx, v4u r) {
        int c = idx >> 5, w = idx & 31, m = w >> 2, g = w & 3;
        v2u o; o.x = r.x; o.y = r.z;
        *reinterpret_cast<v2u*>(mimg + m * MEM_LD + c * 16 + g * 4) = o;
    };
    put(i0, r0); put(i1, r1);
    if (tid < 256) put(i2, r2);
    return true;
}

__device__ __forceinline__ void role_a(const Mega& a, char* smem, int h) {
    const int tid = threadIdx.x, wave = __builtin_amdgcn_readfirstlane(tid >> 6), lane = tid & 63, fr = lane & 15, fq = lane >> 4;
    constexpr int NREG = 44;
    bf16x8 wr[NREG];
    char* lw = smem + L_W + wave * (16 * 1024) + lane * 16;
#pragma unroll
    for (int f = 0; f < 60; ++f) {
        const int t = f / 20, ks = f % 20, tile = 3 * wave + t;
        const int nrow = (tile < 12 ? h * DH + tile * 16 : HD + h * DH + (tile - 12) * 16) + fr;
        bf16x8 v = ld_frag(a.WT + (long)nrow * DM + ks * 32 + fq * 8);
        if (f < NREG) wr[f] = v; else *reinterpret_cast<bf16x8*>(lw + (f - NREG) * 1024) = v;
    }
    u16* mimg = reinterpret_cast<u16*>(smem + L_MEM);
    u16* qk = reinterpret_cast<u16*>(smem + L_QK);
    auto kx_load = [&](int s) -> bool {
        if ((s & 3) == 0) { if (!wave_wait(a.ctl, C_K + (s >> 2), 24u)) return false; }
        const u16* kp = a.QK + (long)(s * 32 + fr) * QLD + HD + h * DH + fq * 8;
#pragma unroll
        for (int t = 0; t < 2; ++t)
#pragma unroll
            for (int ks = 0; ks < 6; ++ks)
                __builtin_amdgcn_global_load_lds((const unsigned*)(kp + (long)t * 16 * QLD + ks * 32),
                                                 (unsigned*)(smem + L_KX + (t * 6 + ks) * 1024), 16, 0, 16);
        return true;
    };
    if (wave == 0) kx_load(0);
    const float scale = 0.07216878364870322f;
    constexpr int STAMPW = 0;
    for (int s = 0; s < NSEG; ++s) {
        if (!gather_mem(a, smem, s, tid)) return;
        STAMP(0);
        __syncthreads();
        if (s > 0 && tid == 0) __hip_atomic_fetch_add(a.ctl + CW(C_CH + s - 1), 1u, __ATOMIC_RELAXED, __HIP_MEMORY_SCOPE_AGENT);
        STAMP(1);
        f32x4 acc[3];
#pragma unroll
        for (int t = 0; t < 3; ++t) acc[t] = (f32x4){0.f, 0.f, 0.f, 0.f};
        const u16* mrow = mimg + (lane & 7) * MEM_LD + fq * 8;
#pragma unroll
        for (int ks = 0; ks < 20; ++ks) {
            bf16x8 mf = *reinterpret_cast<const bf16x8*>(mrow + ks * 32);
#pragma unroll
            for (int t = 0; t < 3; ++t) {
                const int f = t * 20 + ks;
                bf16x8 w;
                if (f < NREG) w = wr[f]; else w = *reinterpret_cast<const bf16x8*>(lw + (f - NREG) * 1024);
                acc[t] = MFMA(w, mf, acc[t]);
            }
        }
        if (fr < 8) {
#pragma unroll
            for (int t = 0; t < 3; ++t) {
                const int tile = 3 * wave + t;
                v2u o; o.x = pack2(acc[t][0], acc[t][1]); o.y = pack2(acc[t][2], acc[t][3]);
                if (tile < 12) {
                    *reinterpret_cast<v2u*>(qk + fr * QK_LD + tile * 16 + fq * 4) = o;
                } else {
                    *reinterpret_cast<v2u*>(qk + (8 + fr) * QK_LD + (tile - 12) * 16 + fq * 4) = o;
                    st_sc1_x2(a.km + (((long)s * NH + h) * 8 + fr) * DH + (tile - 12) * 16 + fq * 4, o);
                }
            }
        }
        STAMP(2);
        __syncthreads();
        STAMP(3);
        if (wave == 0 && s + 1 < NSEG) {
            f32x4 s0 = (f32x4){0.f, 0.f, 0.f, 0.f}, s1 = s0, s2 = s0;
            const u16* qrow = qk + (lane & 7) * QK_LD + fq * 8;
            const u16* krow = qk + (8 + (lane & 7)) * QK_LD + fq * 8;
            const char* kxp = smem + L_KX + lane * 16;
#pragma unroll
            for (int ks = 0; ks < 6; ++ks) {
                bf16x8 qf = *reinterpret_cast<const bf16x8*>(qrow + ks * 32);
                s0 = MFMA(*reinterpret_cast<const bf16x8*>(krow + ks * 32), qf, s0);
                s1 = MFMA(*reinterpret_cast<const bf16x8*>(kxp + ks * 1024), qf, s1);
                s2 = MFMA(*reinterpret_cast<const bf16x8*>(kxp + (6 + ks) * 1024), qf, s2);
            }
            float v[12]; float mx = -3.0e38f;
#pragma unroll
            for (int r = 0; r < 4; ++r) {
                v[r] = (fq < 2) ? bfr(bfr(s0[r]) * scale) : -1.0e9f;
                v[4 + r] = bfr(bfr(s1[r]) * scale);
                v[8 + r] = bfr(bfr(s2[r]) * scale);
            }
#pragma unroll
            for (int j = 0; j < 12; ++j) mx = fmaxf(mx, v[j]);
            mx = max4g(mx);
            float sum = 0.f;
#pragma unroll
            for (int j = 0; j < 12; ++j) { v[j] = (j < 4 && fq >= 2) ? 0.f : __expf(v[j] - mx); sum += v[j]; }
            sum = sum4g(sum);
            const float inv = 1.0f / sum;
            if (fr < 8) {
                v4u* pb = a.pg + ((size_t)(s & 1) * NA + h) * 80 + fr * 10;
                const unsigned tg = (unsigned)(s + 1);
                v4u o; o.y = tg; o.w = tg;
                o.x = pack2(v[4] * inv, v[5] * inv); o.z = pack2(v[6] * inv, v[7] * inv);
                st_sc1_x4(pb + 2 + fq, o);
                o.x = pack2(v[8] * inv, v[9] * inv); o.z = pack2(v[10] * inv, v[11] * inv);
                st_sc1_x4(pb + 6 + fq, o);
                if (fq < 2) {
                    o.x = pack2(v[0] * inv, v[1] * inv); o.z = pack2(v[2] * inv, v[3] * inv);
                    st_sc1_x4(pb + fq, o);
                }
            }
            STAMP(4);
            asm volatile("s_waitcnt lgkmcnt(0)" ::: "memory");
            kx_load(s + 1);
        }
        __builtin_amdgcn_s_barrier();
    }
    wg_signal(a.ctl, C_CH + NSEG - 1);
}

template <int T1, int T2, int NL>
__device__ __forceinline__ void role_b_wave(const Mega& a, char* smem, int h, int lane, int t1_0, int t2_0, int lds_off) {
    const int tid = threadIdx.x, fr = lane & 15, fq = lane >> 4;
    constexpr int NF = T1 * 20 + T2 * 6, NREG = NF - NL;
    bf16x8 wr[NREG];
    char* lw = smem + L_W + lds_off + lane * 16;
#pragma unroll
    for (int f = 0; f < NF; ++f) {
        bf16x8 v;
        if (f < T1 * 20) {
            const int t = f / 20, ks = f % 20;
            v = ld_frag(a.WT + (long)(2 * HD + h * DH + (t1_0 + t) * 16 + fr) * DM + ks * 32 + fq * 8);
        } else {
            const int f2 = f - T1 * 20, t = f2 / 6, ks = f2 % 6;
            v = ld_frag(a.WoT + ((long)h * DM + (t2_0 + t) * 16 + fr) * DH + ks * 32 + fq * 8);
        }
        if (f < NREG) wr[f] = v; else *reinterpret_cast<bf16x8*>(lw + (f - NREG) * 1024) = v;
    }
    u16* mimg = reinterpret_cast<u16*>(smem + L_MEM);
    u16* vimg = reinterpret_cast<u16*>(smem + L_QK);
    for (int s = 0; s < NSEG; ++s) {
        if (!gather_mem(a, smem, s, tid)) return;
        __syncthreads();
        {
            f32x4 acc[T1];
#pragma unroll
            for (int t = 0; t < T1; ++t) acc[t] = (f32x4){0.f, 0.f, 0.f, 0.f};
            const u16* mrow = mimg + (lane & 7) * MEM_LD + fq * 8;
#pragma unroll
            for (int ks = 0; ks < 20; ++ks) {
                bf16x8 mf = *reinterpret_cast<const bf16x8*>(mrow + ks * 32);
#pragma unroll
                for (int t = 0; t < T1; ++t) {
                    const int f = t * 20 + ks;
                    bf16x8 w;
                    if (f < NREG) w = wr[f]; else w = *reinterpret_cast<const bf16x8*>(lw + (f - NREG) * 1024);
                    acc[t] = MFMA(w, mf, acc[t]);
                }
            }
            if (fr < 8) {
#pragma unroll
                for (int t = 0; t < T1; ++t) {
                    v2u o; o.x = pack2(acc[t][0], acc[t][1]); o.y = pack2(acc[t][2], acc[t][3]);
                    *reinterpret_cast<v2u*>(vimg + fr * QK_LD + (t1_0 + t) * 16 + fq * 4) = o;
                }
            }
        }
        __syncthreads();
        {
            f32x4 acc[T2];
#pragma unroll
            for (int t = 0; t < T2; ++t) acc[t] = (f32x4){0.f, 0.f, 0.f, 0.f};
            const u16* vrow = vimg + (lane & 7) * QK_LD + fq * 8;
#pragma unroll
            for (int ks = 0; ks < 6; ++ks) {
                bf16x8 vf = *reinterpret_cast<const bf16x8*>(vrow + ks * 32);
#pragma unroll
                for (int t = 0; t < T2; ++t) {
                    const int f = T1 * 20 + t * 6 + ks;
                    bf16x8 w;
                    if (f < NREG) w = wr[f]; else w = *reinterpret_cast<const bf16x8*>(lw + (f - NREG) * 1024);
                    acc[t] = MFMA(vf, w, acc[t]);
                }
            }
            if (fq < 2) {
#pragma unroll
                for (int t = 0; t < T2; ++t) {
                    v4u o; o.x = pack2(acc[t][0], acc[t][1]); o.y = (unsigned)(s + 1); o.z = pack2(acc[t][2], acc[t][3]); o.w = (unsigned)(s + 1);
                    st_sc1_x4(a.zg + (((size_t)(s & 1) * NC + t2_0 + t) * NB + h) * 32 + fr * 2 + fq, o);
                }
            }
        }
    }
}

__device__ __forceinline__ void role_c(const Mega& a, char* smem, int c, int rg) {
    const int tid = threadIdx.x, wave = __builtin_amdgcn_readfirstlane(tid >> 6), lane = tid & 63, fr = lane & 15, fq = lane >> 4;
    u16* pimg = reinterpret_cast<u16*>(smem + LC_P);
    u16* zimg = reinterpret_cast<u16*>(smem + LC_Z);
    float* red = reinterpret_cast<float*>(smem + LC_RED);
    int pdst[3]; unsigned psrc[3];
#pragma unroll
    for (int r = 0; r < 3; ++r) {
        const int idx = (r < 2 || tid < 256) ? tid + 512 * r : tid;
        const int hh = idx / 40, rem = idx % 40, i = rem / 10, j = rem % 10;
        pdst[r] = i * C_PLD + (j < 2 ? 1024 + hh * 8 + 4 * j : hh * 32 + 4 * (j - 2));
        psrc[r] = (unsigned)((hh * 80 + rg * 40 + rem) * 16);
    }
    int zdst[2];
#pragma unroll
    for (int r = 0; r < 2; ++r) {
        const int idx = tid + 512 * r;
        const int hh = idx >> 5, w = idx & 31;
        zdst[r] = (w >> 1) * Z_LD + hh * 8 + (w & 1) * 4;
    }
    const __amdgpu_buffer_rsrc_t urs = __builtin_amdgcn_make_buffer_rsrc((void*)a.UT, 0, NSEG * NH * DM * 32 * 2, 0x00020000);
    bf16x8 uf[4];
    auto u_load = [&](int s) -> bool {
        if (s == 0) { if (!wave_wait(a.ctl, C_U0, 32u)) return false; }
        else if (s == 2) { if (!wave_wait(a.ctl, C_U, 64u)) return false; }
        else if ((s & 3) == 0) { if (!wave_wait(a.ctl, C_U + (s >> 2), 32u)) return false; }
#pragma unroll
        for (int k = 0; k < 4; ++k) {
            const unsigned off = (unsigned)((((s * NH + wave * 4 + k) * DM + c * 16 + fr) * 32 + fq * 8) * 2);
            v4u t = ld_sc1(urs, off);
            uf[k] = __builtin_bit_cast(bf16x8, t);
        }
        return true;
    };
    if (!u_load(0)) return;
    const __amdgpu_buffer_rsrc_t prs = __builtin_amdgcn_make_buffer_rsrc((void*)a.pg, 0, 2 * NA * 80 * 16, 0x00020000);
    const __amdgpu_buffer_rsrc_t zrs = __builtin_amdgcn_make_buffer_rsrc((void*)a.zg, 0, 2 * NC * NB * 32 * 16, 0x00020000);
    constexpr int STAMPW = 0;
    for (int s = 0; s < NSEG; ++s) {
        const bool last = (s + 1 == NSEG);
        const unsigned pb = (unsigned)((s & 1) * (NA * 80) * 16);
        const unsigned zb = (unsigned)((((s & 1) * NC + c) * (NB * 32) + tid) * 16);
        const unsigned tag = (unsigned)(s + 1);
        v4u r0 = (v4u){0u, last ? tag : 0u, 0u, last ? tag : 0u}, r1 = r0, r2 = r0;
        v4u y0 = (v4u){0u, 0u, 0u, 0u}, y1 = y0;
        unsigned spins = 0;
        for (;;) {
            if (!TAGOK(y0, tag)) y0 = ld_sc1(zrs, zb);
            if (!TAGOK(y1, tag)) y1 = ld_sc1(zrs, zb + 512 * 16);
            if (!TAGOK(r0, tag)) r0 = ld_sc1(prs, pb + psrc[0]);
            if (!TAGOK(r1, tag)) r1 = ld_sc1(prs, pb + psrc[1]);
            if (!TAGOK(r2, tag)) r2 = ld_sc1(prs, pb + psrc[2]);
            asm volatile("" ::: "memory");
            bool ok = TAGOK(r0, tag) & TAGOK(r1, tag) & TAGOK(r2, tag) & TAGOK(y0, tag) & TAGOK(y1, tag);
            if (__all(ok)) break;
            if (++spins > SPIN_CAP) { if (lane == 0) set_abort(a.ctl); return; }
            if ((spins & 1023u) == 0u && ld_ctl(a.ctl + CW(C_ABORT)) != 0u) return;
        }
        asm volatile("s_waitcnt vmcnt(0)" ::: "memory");
        STAMP(0);
        auto put8 = [&](u16* dst, v4u r) { v2u o; o.x = r.x; o.y = r.z; *reinterpret_cast<v2u*>(dst) = o; };
        if (!last) {
            put8(pimg + pdst[0], r0); put8(pimg + pdst[1], r1); put8(pimg + pdst[2], r2);
        }
        put8(zimg + zdst[0], y0); put8(zimg + zdst[1], y1);
        __syncthreads();
        STAMP(1);
        if (!last) {
            f32x4 acc = (f32x4){0.f, 0.f, 0.f, 0.f};
            const u16* prow = pimg + (lane & 3) * C_PLD + fq * 8;
#pragma unroll
            for (int k = 0; k < 4; ++k) {
                bf16x8 pf = *reinterpret_cast<const bf16x8*>(prow + wave * 128 + k * 32);
                acc = MFMA(uf[k], pf, acc);
            }
            {
                bf16x8 pf = *reinterpret_cast<const bf16x8*>(prow + 1024 + wave * 32);
                bf16x8 zf = *reinterpret_cast<const bf16x8*>(zimg + fr * Z_LD + wave * 32 + fq * 8);
                acc = MFMA(zf, pf, acc);
            }
            *reinterpret_cast<f32x4*>(red + (wave * 64 + lane) * 4) = acc;
        }
        v4u zo = *reinterpret_cast<const v4u*>(zimg + (tid >> 5) * Z_LD + (tid & 31) * 8);
        __syncthreads();
        if (!last && wave == 0 && fr < 4) {
            f32x4 t = *reinterpret_cast<const f32x4*>(red + lane * 4);
#pragma unroll
            for (int w = 1; w < 8; ++w) {
                f32x4 u = *reinterpret_cast<const f32x4*>(red + (w * 64 + lane) * 4);
                t[0] += u[0]; t[1] += u[1]; t[2] += u[2]; t[3] += u[3];
            }
            v4u o; o.x = pack2(t[0], t[1]); o.y = tag; o.z = pack2(t[2], t[3]); o.w = tag;
            st_sc1_x4(a.memg + ((size_t)((s + 1) & 1) * NC + c) * 32 + (rg * 4 + fr) * 4 + fq, o);
        }
        STAMP(2);
        if (rg == 0) {
            if (s > 0 && tid == 64) __hip_atomic_fetch_add(a.ctl + CW(C_CH + s - 1), 1u, __ATOMIC_RELAXED, __HIP_MEMORY_SCOPE_AGENT);
            st_sc1_x4(a.ZT + ((long)s * DM + c * 16 + (tid >> 5)) * 256 + (tid & 31) * 8, zo);
        }
        if (s + 2 < NSEG) { if (!u_load(s + 1)) return; }
    }
    if (rg == 0) wg_signal(a.ctl, C_CH + NSEG - 1);
}

__device__ __forceinline__ bool wg_wait(const Mega& a, char* smem, int w0, unsigned t0, int w1, unsigned t1, int w2, unsigned t2) {
    volatile int* bc = reinterpret_cast<volatile int*>(smem + L_BCAST);
    if (threadIdx.x < 64) {
        bool ok = wave_wait(a.ctl, w0, t0);
        if (ok && w1 >= 0) ok = wave_wait(a.ctl, w1, t1);
        if (ok && w2 >= 0) ok = wave_wait(a.ctl, w2, t2);
        __builtin_amdgcn_fence(__ATOMIC_ACQUIRE, "agent");
        asm volatile("s_waitcnt vmcnt(0)" ::: "memory");
        if (threadIdx.x == 0) bc[1] = ok ? 1 : 0;
    }
    __syncthreads();
    const bool r = bc[1] != 0;
    __syncthreads();
    return r;
}

template <int BM, int BN, int WM, int WN>
__device__ __forceinline__ void gemm_core(const u16* __restrict__ A, long lda, const u16* __restrict__ B, long ldb, int K, char* sm,
                                          f32x4 (&acc)[WM / 16][WN / 16], const int tid) {
    constexpr int ROWS = BM + BN, TB = ROWS * 128, NWN = BN / WN, L = ROWS / 64;
    static_assert(ROWS % 64 == 0, "rows");
    const int wave = tid >> 6, lane = tid & 63, fr = lane & 15, fq = lane >> 4;
    const int wr = wave / NWN, wc = wave % NWN;
    auto stage = [&](int kt, int buf) {
#pragma unroll
        for (int i = 0; i < L; ++i) {
            const int b = tid * 16 + i * 8192, r = b >> 7, c = ((b >> 4) & 7) ^ (r & 7);
            const u16* src = (r < BM) ? A + (long)r * lda + kt * 64 + c * 8 : B + (long)(r - BM) * ldb + kt * 64 + c * 8;
            __builtin_amdgcn_global_load_lds((const unsigned*)src, (unsigned*)(sm + buf * TB + b), 16, 0, 0);
        }
    };
#pragma unroll
    for (int m = 0; m < WM / 16; ++m)
#pragma unroll
        for (int n = 0; n < WN / 16; ++n) acc[m][n] = (f32x4){0.f, 0.f, 0.f, 0.f};
    const int nt = K >> 6;
    stage(0, 0);
    stage(1, 1);
    int buf = 0;
    const int sw = fr & 7;
    for (int t = 0; t < nt; ++t) {
        if (t + 1 < nt) {
            if constexpr (L == 6) asm volatile("s_waitcnt vmcnt(6)" ::: "memory");
            else if constexpr (L == 5) asm volatile("s_waitcnt vmcnt(5)" ::: "memory");
            else if constexpr (L == 4) asm volatile("s_waitcnt vmcnt(4)" ::: "memory");
            else if constexpr (L == 3) asm volatile("s_waitcnt vmcnt(3)" ::: "memory");
            else asm volatile("s_waitcnt vmcnt(0)" ::: "memory");
        } else {
            asm volatile("s_waitcnt vmcnt(0)" ::: "memory");
        }
        __builtin_amdgcn_s_barrier();
        if (t + 2 < nt) stage(t + 2, buf == 0 ? 2 : buf - 1);
        const char* sa = sm + buf * TB + (wr * WM + fr) * 128;
        const char* sb = sm + buf * TB + (BM + wc * WN + fr) * 128;
#pragma unroll
        for (int kk = 0; kk < 2; ++kk) {
            const int co = ((kk * 4 + fq) ^ sw) * 16;
            bf16x8 af[WM / 16], bfg[WN / 16];
#pragma unroll
            for (int m = 0; m < WM / 16; ++m) af[m] = *reinterpret_cast<const bf16x8*>(sa + m * 2048 + co);
#pragma unroll
            for (int n = 0; n < WN / 16; ++n) bfg[n] = *reinterpret_cast<const bf16x8*>(sb + n * 2048 + co);
#pragma unroll
            for (int m = 0; m < WM / 16; ++m)
#pragma unroll
                for (int n = 0; n < WN / 16; ++n) acc[m][n] = MFMA(bfg[n], af[m], acc[m][n]);
        }
        buf = (buf == 2) ? 0 : buf + 1;
    }
    asm volatile("s_waitcnt lgkmcnt(0)" ::: "memory");
    __syncthreads();
}

__device__ __forceinline__ void task_qk(const Mega& a, char* smem, int rb, int col0, const int tid) {
    f32x4 acc[4][4];
    gemm_core<128, 256, 64, 64>(a.xn + (long)rb * 128 * DM, DM, a.WT + (long)col0 * DM, DM, DM, smem, acc, tid);
    const int wave = tid >> 6, lane = tid & 63, fr = lane & 15, fq = lane >> 4, wr = wave >> 2, wc = wave & 3;
    u16* st = reinterpret_cast<u16*>(smem);
#pragma unroll
    for (int m = 0; m < 4; ++m)
#pragma unroll
        for (int n = 0; n < 4; ++n) {
            v2u o; o.x = pack2(acc[m][n][0], acc[m][n][1]); o.y = pack2(acc[m][n][2], acc[m][n][3]);
            *reinterpret_cast<v2u*>(st + (wr * 64 + m * 16 + fr) * 264 + wc * 64 + n * 16 + fq * 4) = o;
        }
    __syncthreads();
#pragma unroll
    for (int i = 0; i < 8; ++i) {
        const int id = tid + 512 * i, row = id >> 5, c16 = id & 31;
        v4u v = *reinterpret_cast<const v4u*>(st + row * 264 + c16 * 8);
        st_sc1_x4(a.QK + (long)(rb * 128 + row) * QLD + col0 + c16 * 8, v);
    }
}

#define VU_V 0
#define VU_U (52 * 1024)
template <int NSG>
__device__ __forceinline__ void task_vu(const Mega& a, char* smem, int seg0, int h, const int tid) {
    const int wave = tid >> 6, lane = tid & 63, fr = lane & 15, fq = lane >> 4;
    u16* vimg = reinterpret_cast<u16*>(smem + VU_V);
    u16* ust = reinterpret_cast<u16*>(smem + VU_U);
    {
        f32x4 acc[NSG][3];
        gemm_core<32 * NSG, 192, 16 * NSG, 48>(a.xn + (long)seg0 * 32 * DM, DM, a.WT + (long)(2 * HD + h * DH) * DM, DM, DM, smem, acc, tid);
        const int wr = wave >> 2, wc = wave & 3;
#pragma unroll
        for (int m = 0; m < NSG; ++m)
#pragma unroll
            for (int n = 0; n < 3; ++n) {
                v2u o; o.x = pack2(acc[m][n][0], acc[m][n][1]); o.y = pack2(acc[m][n][2], acc[m][n][3]);
                *reinterpret_cast<v2u*>(vimg + (wr * 16 * NSG + m * 16 + fr) * 200 + wc * 48 + n * 16 + fq * 4) = o;
            }
    }
    bf16x8 wof[5][6];
#pragma unroll
    for (int ct = 0; ct < 5; ++ct)
#pragma unroll
        for (int ks = 0; ks < 6; ++ks)
            wof[ct][ks] = ld_frag(a.WoT + ((long)h * DM + (wave * 5 + ct) * 16 + fr) * DH + ks * 32 + fq * 8);
    __syncthreads();
    for (int sg = 0; sg < NSG; ++sg) {
        f32x4 acc[5][2];
#pragma unroll
        for (int ct = 0; ct < 5; ++ct) { acc[ct][0] = (f32x4){0.f, 0.f, 0.f, 0.f}; acc[ct][1] = acc[ct][0]; }
        const u16* v0 = vimg + (sg * 32 + fr) * 200 + fq * 8;
#pragma unroll
        for (int ks = 0; ks < 6; ++ks) {
            bf16x8 a0 = *reinterpret_cast<const bf16x8*>(v0 + ks * 32);
            bf16x8 a1 = *reinterpret_cast<const bf16x8*>(v0 + 16 * 200 + ks * 32);
#pragma unroll
            for (int ct = 0; ct < 5; ++ct) { acc[ct][0] = MFMA(a0, wof[ct][ks], acc[ct][0]); acc[ct][1] = MFMA(a1, wof[ct][ks], acc[ct][1]); }
        }
#pragma unroll
        for (int ct = 0; ct < 5; ++ct)
#pragma unroll
            for (int rt = 0; rt < 2; ++rt) {
                v2u o; o.x = pack2(acc[ct][rt][0], acc[ct][rt][1]); o.y = pack2(acc[ct][rt][2], acc[ct][rt][3]);
                *reinterpret_cast<v2u*>(ust + ((wave * 5 + ct) * 16 + fr) * 32 + rt * 16 + fq * 4) = o;
            }
        __syncthreads();
        u16* dst = a.UT + ((long)(seg0 + sg) * NH + h) * (DM * 32);
#pragma unroll
        for (int i = 0; i < 5; ++i) {
            const int id = tid + 512 * i;
            v4u v = *reinterpret_cast<const v4u*>(ust + id * 8);
            st_sc1_x4(dst + id * 8, v);
        }
        __syncthreads();
    }
}

__device__ __forceinline__ bool task_p(const Mega& a, char* smem, int seg, int hg, const int tid) {
    const int wave = tid >> 6, lane = tid & 63, fr = lane & 15, fq = lane >> 4;
    const float scale = 0.07216878364870322f;
    const int h = hg * 8 + wave;
    const u16* kmp = a.km + (((long)seg * NH + h) * 8 + (fr & 7)) * DH + fq * 8;
    const u16* kx0 = a.QK + (long)(seg * 32 + fr) * QLD + HD + h * DH + fq * 8;
    const u16* qp = a.QK + (long)(seg * 32 + fr) * QLD + h * DH + fq * 8;
    if (!wg_wait(a, smem, C_Q + (seg >> 2), 24u, C_K + (seg >> 2), 24u, -1, 0u)) return false;
    bf16x8 kmf[6], kf0[6], kf1[6], qf[2][6];
#pragma unroll
    for (int ks = 0; ks < 6; ++ks) {
        qf[0][ks] = ld_frag(qp + ks * 32); kf0[ks] = ld_frag(kx0 + ks * 32);
        kf1[ks] = ld_frag(kx0 + (long)16 * QLD + ks * 32); qf[1][ks] = ld_frag(qp + (long)16 * QLD + ks * 32);
    }
    f32x4 sx[3];
    sx[0] = (f32x4){0.f, 0.f, 0.f, 0.f}; sx[1] = sx[0]; sx[2] = sx[0];
#pragma unroll
    for (int ks = 0; ks < 6; ++ks) {
        sx[0] = MFMA(kf0[ks], qf[0][ks], sx[0]);
        sx[1] = MFMA(kf0[ks], qf[1][ks], sx[1]);
        sx[2] = MFMA(kf1[ks], qf[1][ks], sx[2]);
    }
    if (!wg_wait(a, smem, C_CH + seg, (unsigned)(NA + NC), -1, 0u, -1, 0u)) return false;
#pragma unroll
    for (int ks = 0; ks < 6; ++ks) kmf[ks] = ld_frag(kmp + ks * 32);
#pragma unroll
    for (int qt = 0; qt < 2; ++qt) {
        f32x4 s0 = (f32x4){0.f, 0.f, 0.f, 0.f};
        const f32x4 s1 = sx[qt], s2 = sx[2];
#pragma unroll
        for (int ks = 0; ks < 6; ++ks) s0 = MFMA(kmf[ks], qf[qt][ks], s0);
        const int ql = qt * 16 + fr;
        float v[12]; float mx = -3.0e38f;
#pragma unroll
        for (int r = 0; r < 4; ++r) {
            v[r] = (fq < 2) ? bfr(bfr(s0[r]) * scale) : -1.0e9f;
            v[4 + r] = (fq * 4 + r <= ql) ? bfr(bfr(s1[r]) * scale) : -1.0e9f;
            v[8 + r] = (16 + fq * 4 + r <= ql) ? bfr(bfr(s2[r]) * scale) : -1.0e9f;
        }
#pragma unroll
        for (int j = 0; j < 12; ++j) mx = fmaxf(mx, v[j]);
        mx = max4g(mx);
        float sum = 0.f;
#pragma unroll
        for (int j = 0; j < 12; ++j) { v[j] = (j < 4 && fq >= 2) ? 0.f : __expf(v[j] - mx); sum += v[j]; }
        sum = sum4g(sum);
        const float inv = 1.0f / sum;
        u16* prow = a.Pbuf + ((long)seg * 32 + ql) * 1280;
        if (fq < 2) {
            v2u o; o.x = pack2(v[0] * inv, v[1] * inv); o.y = pack2(v[2] * inv, v[3] * inv);
            st_sc1_x2(prow + 1024 + h * 8 + fq * 4, o);
        }
        v2u o; o.x = pack2(v[4] * inv, v[5] * inv); o.y = pack2(v[6] * inv, v[7] * inv);
        st_sc1_x2(prow + h * 32 + fq * 4, o);
        o.x = pack2(v[8] * inv, v[9] * inv); o.y = pack2(v[10] * inv, v[11] * inv);
        st_sc1_x2(prow + h * 32 + 16 + fq * 4, o);
    }
    return true;
}

#define XP_LD 1288
__device__ __forceinline__ bool task_o(const Mega& a, char* smem, int seg, int cb, const int tid) {
    u16* P = reinterpret_cast<u16*>(smem);
    const int wave = tid >> 6, lane = tid & 63, fr = lane & 15, fq = lane >> 4;
    const int col0 = cb * 128 + wave * 16;
    f32x4 a0 = (f32x4){0.f, 0.f, 0.f, 0.f}, a1 = a0;
    const u16* up = a.UT + ((long)seg * NH * DM + col0 + fr) * 32 + fq * 8;
    const u16* zp = a.ZT + ((long)seg * DM + col0 + fr) * 256 + fq * 8;
    if (!wg_wait(a, smem, C_U + (seg >> 2), seg < 4 ? 64u : 32u, -1, 0u, -1, 0u)) return false;
    bf16x8 uf[32];
#pragma unroll
    for (int k = 0; k < 32; ++k) uf[k] = ld_frag(up + (long)k * (DM * 32));
    if (!wg_wait(a, smem, C_P + seg, 4u, -1, 0u, -1, 0u)) return false;
    bf16x8 zf[8];
#pragma unroll
    for (int k = 0; k < 8; ++k) zf[k] = ld_frag(zp + k * 32);
#pragma unroll
    for (int i = 0; i < 10; ++i) {
        const int id = tid + 512 * i, row = id / 160, c = (id % 160) * 8;
        *reinterpret_cast<v4u*>(P + row * XP_LD + c) = *reinterpret_cast<const v4u*>(a.Pbuf + ((long)seg * 32 + row) * 1280 + c);
    }
    __syncthreads();
    const u16* p0 = P + fr * XP_LD + fq * 8;
    const u16* p1 = P + (16 + fr) * XP_LD + fq * 8;
#pragma unroll
    for (int ks = 0; ks < 32; ++ks) {
        a0 = MFMA(uf[ks], *reinterpret_cast<const bf16x8*>(p0 + ks * 32), a0);
        a1 = MFMA(uf[ks], *reinterpret_cast<const bf16x8*>(p1 + ks * 32), a1);
    }
#pragma unroll
    for (int ks = 0; ks < 8; ++ks) {
        a0 = MFMA(zf[ks], *reinterpret_cast<const bf16x8*>(p0 + 1024 + ks * 32), a0);
        a1 = MFMA(zf[ks], *reinterpret_cast<const bf16x8*>(p1 + 1024 + ks * 32), a1);
    }
#pragma unroll
    for (int qt = 0; qt < 2; ++qt) {
        const f32x4 av = qt ? a1 : a0;
        const long off = (long)(seg * 32 + qt * 16 + fr) * DM + col0 + fq * 4;
        v2u xi = *reinterpret_cast<const v2u*>(a.x + off);
        v2u o;
        o.x = pack2(bf2f((u16)(xi.x & 0xffff)) + bfr(av[0]), bf2f((u16)(xi.x >> 16)) + bfr(av[1]));
        o.y = pack2(bf2f((u16)(xi.y & 0xffff)) + bfr(av[2]), bf2f((u16)(xi.y >> 16)) + bfr(av[3]));
        st_sc1_x2(a.hbuf + off, o);
    }
    return true;
}

__device__ __forceinline__ float gelu_tanh(float x) {
    const float u = 0.7978845608028654f * (x + 0.044715f * x * x * x);
    return __fdividef(x, 1.0f + __expf(-2.0f * u));
}
#define M1_A 0
#define M1_B (82 * 1024)
#define M1_ST 0
__device__ __forceinline__ bool task_mlp1(const Mega& a, char* smem, int b64, int ct, const int tid) {
    const int wave = tid >> 6, lane = tid & 63, fr = lane & 15, fq = lane >> 4, wr = wave >> 2, wc = wave & 3;
    u16* aimg = reinterpret_cast<u16*>(smem + M1_A);
    const u16* Bg = a.w1T + (long)ct * 128 * DM;
    auto stage = [&](int kt) {
        const int b = tid * 16, r = b >> 6, c = ((b >> 4) & 3) ^ ((r >> 2) & 3);
        __builtin_amdgcn_global_load_lds((const unsigned*)(Bg + (long)r * DM + kt * 32 + c * 8), (unsigned*)(smem + M1_B + (kt & 7) * 8192 + b), 16, 0, 0);
    };
#pragma unroll
    for (int i = 0; i < 7; ++i) stage(i);
    if (!wg_wait(a, smem, C_X + b64, 10u, -1, 0u, -1, 0u)) return false;
#pragma unroll
    for (int i = 0; i < 10; ++i) {
        const int id = tid + 512 * i, row = id / 80, c = (id % 80) * 8;
        *reinterpret_cast<v4u*>(aimg + row * MEM_LD + c) = *reinterpret_cast<const v4u*>(a.hbuf + (long)(b64 * 64 + row) * DM + c);
    }
    __syncthreads();
#pragma unroll
    for (int ps = 0; ps < 2; ++ps) {
        u16* rp = aimg + (wave * 8 + ps * 4 + fq) * MEM_LD + fr * 8;
        float v[40];
#pragma unroll
        for (int j = 0; j < 5; ++j) {
            v4u t = *reinterpret_cast<const v4u*>(rp + j * 128);
            v[j * 8 + 0] = __uint_as_float(t.x << 16); v[j * 8 + 1] = __uint_as_float(t.x & 0xffff0000u);
            v[j * 8 + 2] = __uint_as_float(t.y << 16); v[j * 8 + 3] = __uint_as_float(t.y & 0xffff0000u);
            v[j * 8 + 4] = __uint_as_float(t.z << 16); v[j * 8 + 5] = __uint_as_float(t.z & 0xffff0000u);
            v[j * 8 + 6] = __uint_as_float(t.w << 16); v[j * 8 + 7] = __uint_as_float(t.w & 0xffff0000u);
        }
#pragma unroll
        for (int rep = 0; rep < 2; ++rep) {
            float sm = 0.f;
#pragma unroll
            for (int i = 0; i < 40; ++i) sm += v[i];
            sm += __shfl_xor(sm, 1); sm += __shfl_xor(sm, 2); sm += __shfl_xor(sm, 4); sm += __shfl_xor(sm, 8);
            const float mean = sm * (1.0f / DM);
            float q = 0.f;
#pragma unroll
            for (int i = 0; i < 40; ++i) { const float d = v[i] - mean; q += d * d; }
            q += __shfl_xor(q, 1); q += __shfl_xor(q, 2); q += __shfl_xor(q, 4); q += __shfl_xor(q, 8);
            const float mb = bfr(mean);
            const float rden = 1.0f / bfr(sqrtf(bfr(bfr(q * (1.0f / DM)) + 1e-5f)));
#pragma unroll
            for (int i = 0; i < 40; ++i) v[i] = bfr(bfr(v[i] - mb) * rden);
        }
#pragma unroll
        for (int j = 0; j < 5; ++j) {
            v4u t;
            t.x = pack2(v[j * 8 + 0], v[j * 8 + 1]); t.y = pack2(v[j * 8 + 2], v[j * 8 + 3]);
            t.z = pack2(v[j * 8 + 4], v[j * 8 + 5]); t.w = pack2(v[j * 8 + 6], v[j * 8 + 7]);
            *reinterpret_cast<v4u*>(rp + j * 128) = t;
        }
    }
    f32x4 acc[2][2];
#pragma unroll
    for (int m = 0; m < 2; ++m)
#pragma unroll
        for (int n = 0; n < 2; ++n) acc[m][n] = (f32x4){0.f, 0.f, 0.f, 0.f};
    const int sw = (fq ^ ((fr >> 2) & 3)) * 16;
    for (int t = 0; t < 20; ++t) {
        if (t + 6 < 20) asm volatile("s_waitcnt vmcnt(6) lgkmcnt(0)" ::: "memory");
        else asm volatile("s_waitcnt vmcnt(0) lgkmcnt(0)" ::: "memory");
        __builtin_amdgcn_s_barrier();
        if (t + 7 < 20) stage(t + 7);
        bf16x8 af[2], bfg[2];
#pragma unroll
        for (int m = 0; m < 2; ++m) af[m] = *reinterpret_cast<const bf16x8*>(aimg + (wr * 32 + m * 16 + fr) * MEM_LD + t * 32 + fq * 8);
#pragma unroll
        for (int n = 0; n < 2; ++n) bfg[n] = *reinterpret_cast<const bf16x8*>(smem + M1_B + (t & 7) * 8192 + (wc * 32 + n * 16 + fr) * 64 + sw);
#pragma unroll
        for (int m = 0; m < 2; ++m)
#pragma unroll
            for (int n = 0; n < 2; ++n) acc[m][n] = MFMA(bfg[n], af[m], acc[m][n]);
    }
    asm volatile("s_waitcnt lgkmcnt(0)" ::: "memory");
    __syncthreads();
    u16* st = reinterpret_cast<u16*>(smem + M1_ST);
#pragma unroll
    for (int n = 0; n < 2; ++n) {
        const int cl = wc * 32 + n * 16 + fq * 4;
        v2u bb = *reinterpret_cast<const v2u*>(a.b1 + ct * 128 + cl);
        const float b0 = bf2f((u16)(bb.x & 0xffff)), b1 = bf2f((u16)(bb.x >> 16)), b2 = bf2f((u16)(bb.y & 0xffff)), b3 = bf2f((u16)(bb.y >> 16));
#pragma unroll
        for (int m = 0; m < 2; ++m) {
            v2u o;
            o.x = pack2(gelu_tanh(bfr(bfr(acc[m][n][0]) + b0)), gelu_tanh(bfr(bfr(acc[m][n][1]) + b1)));
            o.y = pack2(gelu_tanh(bfr(bfr(acc[m][n][2]) + b2)), gelu_tanh(bfr(bfr(acc[m][n][3]) + b3)));
            *reinterpret_cast<v2u*>(st + (wr * 32 + m * 16 + fr) * 136 + cl) = o;
        }
    }
    __syncthreads();
#pragma unroll
    for (int i = 0; i < 2; ++i) {
        const int id = tid + 512 * i, row = id >> 4, c16 = id & 15;
        v4u v = *reinterpret_cast<const v4u*>(st + row * 136 + c16 * 8);
        st_sc1_x4(a.hid + (long)(b64 * 64 + row) * FF + ct * 128 + c16 * 8, v);
    }
    return true;
}

__device__ __forceinline__ void task_mlp2(const Mega& a, char* smem, int b64, int ct, const int tid) {
    f32x4 acc[2][2];
    gemm_core<64, 128, 32, 32>(a.hid + (long)b64 * 64 * FF, FF, a.w2T + (long)ct * 128 * FF, FF, FF, smem, acc, tid);
    const int wave = tid >> 6, lane = tid & 63, fr = lane & 15, fq = lane >> 4, wr = wave >> 2, wc = wave & 3;
#pragma unroll
    for (int n = 0; n < 2; ++n) {
        const int col = ct * 128 + wc * 32 + n * 16 + fq * 4;
        v2u bb = *reinterpret_cast<const v2u*>(a.b2 + col);
        const float b0 = bf2f((u16)(bb.x & 0xffff)), b1 = bf2f((u16)(bb.x >> 16)), b2 = bf2f((u16)(bb.y & 0xffff)), b3 = bf2f((u16)(bb.y >> 16));
#pragma unroll
        for (int m = 0; m < 2; ++m) {
            const long off = (long)(b64 * 64 + wr * 32 + m * 16 + fr) * DM + col;
            v2u hi = *reinterpret_cast<const v2u*>(a.hbuf + off);
            v2u o;
            o.x = pack2(bf2f((u16)(hi.x & 0xffff)) + bfr(bfr(acc[m][n][0]) + b0), bf2f((u16)(hi.x >> 16)) + bfr(bfr(acc[m][n][1]) + b1));
            o.y = pack2(bf2f((u16)(hi.y & 0xffff)) + bfr(bfr(acc[m][n][2]) + b2), bf2f((u16)(hi.y >> 16)) + bfr(bfr(acc[m][n][3]) + b3));
            *reinterpret_cast<v2u*>(a.out + off) = o;
        }
    }
}

#define T_P1 928
#define T_P2 (T_P1 + 384)
#define T_END (T_P2 + 35 * 35)
__device__ __forceinline__ void worker(const Mega& a, char* smem) {
    volatile int* bc = reinterpret_cast<volatile int*>(smem + L_BCAST);
    for (;;) {
        if (threadIdx.x == 0) bc[0] = (int)__hip_atomic_fetch_add(a.ctl + CW(C_TICKET), 1u, __ATOMIC_RELAXED, __HIP_MEMORY_SCOPE_AGENT);
        __syncthreads();
        const int t = __builtin_amdgcn_readfirstlane(bc[0]);
        __syncthreads();
        if (t >= T_END) return;
        int tid = threadIdx.x;
        asm volatile("" : "+v"(tid));
        if (t < T_P1) {
            if (t < 88) {
                if (t < 32) { task_vu<2>(a, smem, 0, t, tid); wg_signal(a.ctl, C_U0); if (threadIdx.x == 0) __hip_atomic_fetch_add(a.ctl + CW(C_U), 1u, __ATOMIC_RELAXED, __HIP_MEMORY_SCOPE_AGENT); }
                else if (t < 56) { task_qk(a, smem, 0, HD + (t - 32) * 256, tid); wg_signal(a.ctl, C_K); }
                else { task_vu<2>(a, smem, 2, t - 56, tid); wg_signal(a.ctl, C_U); }
            } else {
                const int rb = (t - 32) / 56, r = (t - 32) % 56;
                if (r < 32) { task_vu<4>(a, smem, rb * 4, r, tid); wg_signal(a.ctl, C_U + rb); }
                else { task_qk(a, smem, rb, HD + (r - 32) * 256, tid); wg_signal(a.ctl, C_K + rb); }
            }
        } else if (t < T_P2) {
            const int u = t - T_P1, rb = u / 24, ct = u % 24;
            task_qk(a, smem, rb, ct * 256, tid); wg_signal(a.ctl, C_Q + rb);
        } else {
            const int u = t - T_P2, g = u / 35, r = u % 35;
            if (r < 8) {
                const int b = g;
                if (b < 32) {
                    const int seg = 2 * b + (r >> 2);
                    if (!task_p(a, smem, seg, r & 3, tid)) return;
                    wg_signal(a.ctl, C_P + seg);
                }
            } else if (r < 18) {
                const int b = g - 1;
                if (b >= 0 && b < 32) {
                    const int seg = 2 * b + (r - 8) / 5;
                    if (!task_o(a, smem, seg, (r - 8) % 5, tid)) return;
                    wg_signal(a.ctl, C_X + b);
                }
            } else if (r < 30) {
                const int b = g - 2;
                if (b >= 0 && b < 32) {
                    if (!task_mlp1(a, smem, b, r - 18, tid)) return;
                    wg_signal(a.ctl, C_M + b);
                }
            } else {
                const int b = g - 3;
                if (b >= 0 && b < 32) {
                    if (!wg_wait(a, smem, C_M + b, 12u, -1, 0u, -1, 0u)) return;
                    task_mlp2(a, smem, b, r - 30, tid);
                }
            }
        }
        __syncthreads();
    }
}

__global__ __launch_bounds__(512, 2) void k_mega(Mega a) {
    __shared__ __attribute__((aligned(16))) char smem[160 * 1024];
    const int b = blockIdx.x;
    if (b < NA) {
        role_a(a, smem, b);
    } else if (b < NA + NB) {
        const int h = b - NA;
        const int wave = __builtin_amdgcn_readfirstlane(threadIdx.x >> 6), lane = threadIdx.x & 63;
        if (wave < 4) role_b_wave<2, 4, 21>(a, smem, h, lane, 2 * wave, 4 * wave, wave * 21 * 1024);
        else role_b_wave<1, 6, 13>(a, smem, h, lane, 8 + (wave - 4), 16 + 6 * (wave - 4), (84 + (wave - 4) * 13) * 1024);
    } else if (b < NCHAIN) {
        role_c(a, smem, (b - NA - NB) >> 1, (b - NA - NB) & 1);
    } else {
        worker(a, smem);
    }
}

extern "C" void kernel_launch(void* const* d_in, const int* in_sizes, int n_in, void* d_out, int out_size, void* d_ws,
                              size_t ws_size, hipStream_t stream) {
    char* ws = (char*)d_ws;
    size_t off = 0;
    auto take = [&](size_t bytes) { char* p = ws + off; off += (bytes + 255) & ~(size_t)255; return p; };
    const size_t gran_bytes = (size_t)(2 * NC * 32 + 2 * NA * 80 + 2 * NC * NB * 32) * 16;
    const size_t comm_bytes = gran_bytes + (size_t)C_NWORDS * 64;
    char* comm = take(comm_bytes);
    u16* xn = (u16*)take((size_t)NTOK * DM * 2);
    u16* WT = (u16*)take((size_t)3 * HD * DM * 2);
    u16* WoT = (u16*)take((size_t)NH * DM * DH * 2);
    u16* w1T = (u16*)take((size_t)FF * DM * 2);
    u16* w2T = (u16*)take((size_t)DM * FF * 2);
    u16* QK = (u16*)take((size_t)NTOK * QLD * 2);
    u16* UT = (u16*)take((size_t)NSEG * NH * DM * 32 * 2);
    u16* km = (u16*)take((size_t)NSEG * NH * 8 * DH * 2);
    u16* ZT = (u16*)take((size_t)NSEG * DM * 256 * 2);
    u16* hbuf = (u16*)take((size_t)NTOK * DM * 2);
    u16* hid = (u16*)take((size_t)NTOK * FF * 2);
    u16* Pbuf = (u16*)take((size_t)NTOK * 1280 * 2);

    PreArgs p;
    p.Wq = (const u16*)d_in[2]; p.Wk = (const u16*)d_in[3]; p.Wv = (const u16*)d_in[4]; p.Wo = (const u16*)d_in[5];
    p.w1 = (const u16*)d_in[8]; p.w2 = (const u16*)d_in[10]; p.x = (const u16*)d_in[0];
    p.WT = WT; p.WoT = WoT; p.w1T = w1T; p.w2T = w2T; p.xn = xn;
    p.zero = (v4u*)comm; p.nzero16 = (long)(comm_bytes / 16);
    k_pre<<<PRE_BLOCKS, 256, 0, stream>>>(p);

    Mega a;
    a.x = (const u16*)d_in[0]; a.mem0 = (const u16*)d_in[1]; a.b1 = (const u16*)d_in[9]; a.b2 = (const u16*)d_in[11];
    a.xn = xn; a.WT = WT; a.WoT = WoT; a.w1T = w1T; a.w2T = w2T;
    a.QK = QK; a.UT = UT; a.km = km; a.ZT = ZT; a.hbuf = hbuf; a.hid = hid; a.out = (u16*)d_out; a.Pbuf = Pbuf;
    a.memg = (v4u*)comm; a.pg = a.memg + 2 * NC * 32; a.zg = a.pg + 2 * NA * 80;
    a.ctl = (unsigned*)(comm + gran_bytes);
    k_mega<<<256, 512, 0, stream>>>(a);
}
```

```cpp
#include <hip/hip_runtime.h>
#include <hip/hip_bf16.h>

typedef unsigned short u16;
typedef unsigned long long u64;
using bf16x8 = __attribute__((ext_vector_type(8))) short;
using f32x4 = __attribute__((ext_vector_type(4))) float;
typedef unsigned v4u __attribute__((ext_vector_type(4)));
typedef unsigned v2u __attribute__((ext_vector_type(2)));

#define NSEG 64
#define DM 640
#define NH 32
#define DH 192
#define HD 6144
#define QLD 12288
#define NTOK 2048
#define FF 1536

__device__ __forceinline__ float bf2f(u16 v) { return __uint_as_float(((unsigned)v) << 16); }
__device__ __forceinline__ u16 f2bf(float f) {
    __hip_bfloat16 h = __float2bfloat16(f);
    return *reinterpret_cast<u16*>(&h);
}
__device__ __forceinline__ float bfr(float f) { return bf2f(f2bf(f)); }
__device__ __forceinline__ unsigned pack2(float a, float b) { return (unsigned)f2bf(a) | ((unsigned)f2bf(b) << 16); }
__device__ __forceinline__ bf16x8 ld_frag(const u16* p) { return *reinterpret_cast<const bf16x8*>(p); }
#define MFMA(a, b, c) __builtin_amdgcn_mfma_f32_16x16x32_bf16((a), (b), (c), 0, 0, 0)
__device__ __forceinline__ float max4g(float x) {
    auto r = __builtin_amdgcn_permlane16_swap(__float_as_uint(x), __float_as_uint(x), false, false);
    x = fmaxf(__uint_as_float(r[0]), __uint_as_float(r[1]));
    auto q = __builtin_amdgcn_permlane32_swap(__float_as_uint(x), __float_as_uint(x), false, false);
    return fmaxf(__uint_as_float(q[0]), __uint_as_float(q[1]));
}
__device__ __forceinline__ float sum4g(float x) {
    auto r = __builtin_amdgcn_permlane16_swap(__float_as_uint(x), __float_as_uint(x), false, false);
    x = __uint_as_float(r[0]) + __uint_as_float(r[1]);
    auto q = __builtin_amdgcn_permlane32_swap(__float_as_uint(x), __float_as_uint(x), false, false);
    return __uint_as_float(q[0]) + __uint_as_float(q[1]);
}

__device__ __forceinline__ float wave_sum(float v) {
#pragma unroll
    for (int m = 32; m >= 1; m >>= 1) v += __shfl_xor(v, m);
    return v;
}
__device__ __forceinline__ void ln_row(float (&v)[10]) {
    float s = 0.f;
#pragma unroll
    for (int i = 0; i < 10; ++i) s += v[i];
    s = wave_sum(s);
    float mean = s * (1.0f / DM);
    float q = 0.f;
#pragma unroll
    for (int i = 0; i < 10; ++i) { float d = v[i] - mean; q += d * d; }
    q = wave_sum(q);
    float mb = bfr(mean);
    float vb = bfr(q * (1.0f / DM));
    float den = bfr(sqrtf(bfr(vb + 1e-5f)));
#pragma unroll
    for (int i = 0; i < 10; ++i) v[i] = bfr(bfr(v[i] - mb) / den);
}

struct PreArgs {
    const u16 *Wq, *Wk, *Wv, *Wo, *w1, *w2, *x;
    u16 *WT, *WoT, *w1T, *w2T, *xn;
    v4u* zero; long nzero16;
};
__device__ __forceinline__ void tr_tile(const u16* ib, u16* ob, int R, int C, int r0, int c0, u16 (*t)[72]) {
    const int tid = threadIdx.x;
#pragma unroll
    for (int i = 0; i < 2; ++i) {
        const int id = tid + 256 * i, r = id >> 3, c = (id & 7) * 8;
        const v4u v = *reinterpret_cast<const v4u*>(ib + (long)(r0 + r) * C + c0 + c);
        t[c + 0][r] = (u16)(v.x & 0xffff); t[c + 1][r] = (u16)(v.x >> 16);
        t[c + 2][r] = (u16)(v.y & 0xffff); t[c + 3][r] = (u16)(v.y >> 16);
        t[c + 4][r] = (u16)(v.z & 0xffff); t[c + 5][r] = (u16)(v.z >> 16);
        t[c + 6][r] = (u16)(v.w & 0xffff); t[c + 7][r] = (u16)(v.w >> 16);
    }
    __syncthreads();
#pragma unroll
    for (int i = 0; i < 2; ++i) {
        const int id = tid + 256 * i, c = id >> 3, r = (id & 7) * 8;
        *reinterpret_cast<v4u*>(ob + (long)(c0 + c) * R + r0 + r) = *reinterpret_cast<const v4u*>(&t[c][r]);
    }
}
__global__ __launch_bounds__(256) void k_pre(PreArgs p) {
    __shared__ __attribute__((aligned(16))) u16 t[64][72];
    int b = blockIdx.x;
    if (b < 512) {
        int row = b * 4 + (threadIdx.x >> 6), lane = threadIdx.x & 63;
        float v[10];
#pragma unroll
        for (int i = 0; i < 10; ++i) v[i] = bf2f(p.x[(long)row * DM + lane + 64 * i]);
        ln_row(v);
#pragma unroll
        for (int i = 0; i < 10; ++i) p.xn[(long)row * DM + lane + 64 * i] = f2bf(v[i]);
        return;
    }
    b -= 512;
    if (b < 2880) {
        int w = b / 960, r = b % 960;
        const u16* src = w == 0 ? p.Wq : (w == 1 ? p.Wk : p.Wv);
        tr_tile(src, p.WT + (size_t)w * HD * DM, DM, HD, (r % 10) * 64, (r / 10) * 64, t);
        return;
    }
    b -= 2880;
    if (b < 960) {
        int h = b / 30, r = b % 30;
        tr_tile(p.Wo + (size_t)h * DH * DM, p.WoT + (size_t)h * DM * DH, DH, DM, (r % 3) * 64, (r / 3) * 64, t);
        return;
    }
    b -= 960;
    if (b < 240) {
        tr_tile(p.w1, p.w1T, DM, FF, (b % 10) * 64, (b / 10) * 64, t);
        return;
    }
    b -= 240;
    if (b < 240) {
        tr_tile(p.w2, p.w2T, FF, DM, (b % 24) * 64, (b / 24) * 64, t);
        return;
    }
    b -= 240;
    for (long i = (long)b * 256 + threadIdx.x; i < p.nzero16; i += 128 * 256) p.zero[i] = (v4u){0u, 0u, 0u, 0u};
}
#define PRE_BLOCKS (512 + 2880 + 960 + 240 + 240 + 128)

#define NA 32
#define NB 32
#define NC 40
#define NCHAIN (NA + NB + 2 * NC)
#define SPIN_CAP 3000000u

struct Mega {
    const u16 *x, *mem0, *b1, *b2;
    const u16 *xn, *WT, *WoT, *w1T, *w2T;
    u16 *QK, *UT, *km, *ZT, *hbuf, *hid, *out, *Pbuf;
    v4u* memg;
    v4u* pg;
    v4u* zg;
    unsigned* ctl;
};
#define CW(i) ((i) * 16)
#define C_ABORT 0
#define C_TICKET 1
#define C_K 2
#define C_U 18
#define C_Q 34
#define C_CH 50
#define C_X 114
#define C_M 146
#define C_P 178
#define C_U0 242
#define C_NWORDS 243

#define STAMP(slot) do {} while (0)

#define MEM_LD 656
#define QK_LD 200
#define L_W 0
#define L_KX (128 * 1024)
#define L_MEM (140 * 1024)
#define L_QK (L_MEM + 10496)
#define L_END (L_QK + 6400)
static_assert(L_END <= 160 * 1024 - 64, "lds");
#define C_PLD 1288
#define Z_LD 264
#define LC_P 0
#define LC_Z (LC_P + 20608)
#define LC_RED (LC_Z + 8448)
#define L_BCAST (160 * 1024 - 64)

__device__ __forceinline__ void st_sc1_x4(void* p, v4u v) {
    asm volatile("global_store_dwordx4 %0, %1, off sc1\n\ts_nop 1" ::"v"(p), "v"(v) : "memory");
}
__device__ __forceinline__ void st_sc1_x2(void* p, v2u v) {
    asm volatile("global_store_dwordx2 %0, %1, off sc1\n\ts_nop 0" ::"v"(p), "v"(v) : "memory");
}
__device__ __forceinline__ v4u ld_sc1(__amdgpu_buffer_rsrc_t r, unsigned off) { return __builtin_amdgcn_raw_buffer_load_b128(r, off, 0, 16); }
__device__ __forceinline__ v4u ld_sc1p(const void* base, unsigned off) {
    return __builtin_amdgcn_raw_buffer_load_b128(__builtin_amdgcn_make_buffer_rsrc((void*)base, 0, 0x7fffffff, 0x00020000), off, 0, 16);
}
__device__ __forceinline__ v2u ld_sc1p2(const void* base, unsigned off) {
    return __builtin_amdgcn_raw_buffer_load_b64(__builtin_amdgcn_make_buffer_rsrc((void*)base, 0, 0x7fffffff, 0x00020000), off, 0, 16);
}
#define TAGOK(r, tag) (((r).y == (tag)) & ((r).w == (tag)))
__device__ __forceinline__ unsigned ld_ctl(unsigned* p) {
    return (unsigned)__builtin_amdgcn_readfirstlane(__hip_atomic_load(p, __ATOMIC_RELAXED, __HIP_MEMORY_SCOPE_AGENT));
}
__device__ __forceinline__ void set_abort(unsigned* ctl) { __hip_atomic_store(ctl + CW(C_ABORT), 1u, __ATOMIC_RELAXED, __HIP_MEMORY_SCOPE_AGENT); }
__device__ __forceinline__ bool wave_wait(unsigned* ctl, int word, unsigned target) {
    unsigned spins = 0;
    while (ld_ctl(ctl + CW(word)) < target) {
        __builtin_amdgcn_s_sleep(4);
        if (++spins > SPIN_CAP) { set_abort(ctl); return false; }
        if ((spins & 255u) == 0u && ld_ctl(ctl + CW(C_ABORT)) != 0u) return false;
    }
    return true;
}
__device__ __forceinline__ void wg_signal(unsigned* ctl, int word) {
    asm volatile("s_waitcnt vmcnt(0)" ::: "memory");
    __syncthreads();
    if (threadIdx.x == 0) __hip_atomic_fetch_add(ctl + CW(word), 1u, __ATOMIC_RELAXED, __HIP_MEMORY_SCOPE_AGENT);
}

__device__ __forceinline__ bool gather_mem(const Mega& a, char* smem, int s, int tid) {
    u16* mimg = reinterpret_cast<u16*>(smem + L_MEM);
    if (s == 0) {
        for (int i = tid; i < 8 * 80; i += 512) {
            int m = i / 80, c = (i % 80) * 8;
            *reinterpret_cast<bf16x8*>(mimg + m * MEM_LD + c) = ld_frag(a.mem0 + m * DM + c);
        }
        asm volatile("s_waitcnt vmcnt(0)" ::: "memory");
        return true;
    }
    const __amdgpu_buffer_rsrc_t rs = __builtin_amdgcn_make_buffer_rsrc((void*)a.memg, 0, 2 * NC * 32 * 16, 0x00020000);
    const int i0 = tid, i1 = tid + 512, i2 = (tid < 256) ? tid + 1024 : tid;
    const unsigned ob = (unsigned)(s & 1) * (NC * 32 * 16);
    v4u r0 = (v4u){0u, 0u, 0u, 0u}, r1 = r0, r2 = r0;
    unsigned spins = 0;
    const unsigned tag = (unsigned)s;
    for (;;) {
        if (!TAGOK(r0, tag)) r0 = ld_sc1(rs, ob + i0 * 16);
        if (!TAGOK(r1, tag)) r1 = ld_sc1(rs, ob + i1 * 16);
        if (!TAGOK(r2, tag)) r2 = ld_sc1(rs, ob + i2 * 16);
        asm volatile("" ::: "memory");
        bool ok = TAGOK(r0, tag) & TAGOK(r1, tag) & TAGOK(r2, tag);
        if (__all(ok)) break;
        if (++spins > SPIN_CAP) { if ((tid & 63) == 0) set_abort(a.ctl); return false; }
        if ((spins & 1023u) == 0u && ld_ctl(a.ctl + CW(C_ABORT)) != 0u) return false;
    }
    asm volatile("s_waitcnt vmcnt(0)" ::: "memory");
    auto put = [&](int idx, v4u r) {
        int c = idx >> 5, w = idx & 31, m = w >> 2, g = w & 3;
        v2u o; o.x = r.x; o.y = r.z;
        *reinterpret_cast<v2u*>(mimg + m * MEM_LD + c * 16 + g * 4) = o;
    };
    put(i0, r0); put(i1, r1);
    if (tid < 256) put(i2, r2);
    return true;
}

__device__ __forceinline__ void role_a(const Mega& a, char* smem, int h) {
    const int tid = threadIdx.x, wave = __builtin_amdgcn_readfirstlane(tid >> 6), lane = tid & 63, fr = lane & 15, fq = lane >> 4;
    constexpr int NREG = 44;
    bf16x8 wr[NREG];
    char* lw = smem + L_W + wave * (16 * 1024) + lane * 16;
#pragma unroll
    for (int f = 0; f < 60; ++f) {
        const int t = f / 20, ks = f % 20, tile = 3 * wave + t;
        const int nrow = (tile < 12 ? h * DH + tile * 16 : HD + h * DH + (tile - 12) * 16) + fr;
        bf16x8 v = ld_frag(a.WT + (long)nrow * DM + ks * 32 + fq * 8);
        if (f < NREG) wr[f] = v; else *reinterpret_cast<bf16x8*>(lw + (f - NREG) * 1024) = v;
    }
    u16* mimg = reinterpret_cast<u16*>(smem + L_MEM);
    u16* qk = reinterpret_cast<u16*>(smem + L_QK);
    auto kx_load = [&](int s) -> bool {
        if ((s & 3) == 0) { if (!wave_wait(a.ctl, C_K + (s >> 2), 24u)) return false; }
        const u16* kp = a.QK + (long)(s * 32 + fr) * QLD + HD + h * DH + fq * 8;
#pragma unroll
        for (int t = 0; t < 2; ++t)
#pragma unroll
            for (int ks = 0; ks < 6; ++ks)
                __builtin_amdgcn_global_load_lds((const unsigned*)(kp + (long)t * 16 * QLD + ks * 32),
                                                 (unsigned*)(smem + L_KX + (t * 6 + ks) * 1024), 16, 0, 16);
        return true;
    };
    if (wave == 0) kx_load(0);
    const float scale = 0.07216878364870322f;
    constexpr int STAMPW = 0;
    for (int s = 0; s < NSEG; ++s) {
        if (!gather_mem(a, smem, s, tid)) return;
        STAMP(0);
        __syncthreads();
        if (s > 0 && tid == 0) __hip_atomic_fetch_add(a.ctl + CW(C_CH + s - 1), 1u, __ATOMIC_RELAXED, __HIP_MEMORY_SCOPE_AGENT);
        STAMP(1);
        f32x4 acc[3];
#pragma unroll
        for (int t = 0; t < 3; ++t) acc[t] = (f32x4){0.f, 0.f, 0.f, 0.f};
        const u16* mrow = mimg + (lane & 7) * MEM_LD + fq * 8;
#pragma unroll
        for (int ks = 0; ks < 20; ++ks) {
            bf16x8 mf = *reinterpret_cast<const bf16x8*>(mrow + ks * 32);
#pragma unroll
            for (int t = 0; t < 3; ++t) {
                const int f = t * 20 + ks;
                bf16x8 w;
                if (f < NREG) w = wr[f]; else w = *reinterpret_cast<const bf16x8*>(lw + (f - NREG) * 1024);
                acc[t] = MFMA(w, mf, acc[t]);
            }
        }
        if (fr < 8) {
#pragma unroll
            for (int t = 0; t < 3; ++t) {
                const int tile = 3 * wave + t;
                v2u o; o.x = pack2(acc[t][0], acc[t][1]); o.y = pack2(acc[t][2], acc[t][3]);
                if (tile < 12) {
                    *reinterpret_cast<v2u*>(qk + fr * QK_LD + tile * 16 + fq * 4) = o;
                } else {
                    *reinterpret_cast<v2u*>(qk + (8 + fr) * QK_LD + (tile - 12) * 16 + fq * 4) = o;
                    st_sc1_x2(a.km + (((long)s * NH + h) * 8 + fr) * DH + (tile - 12) * 16 + fq * 4, o);
                }
            }
        }
        STAMP(2);
        __syncthreads();
        STAMP(3);
        if (wave == 0 && s + 1 < NSEG) {
            f32x4 s0 = (f32x4){0.f, 0.f, 0.f, 0.f}, s1 = s0, s2 = s0;
            const u16* qrow = qk + (lane & 7) * QK_LD + fq * 8;
            const u16* krow = qk + (8 + (lane & 7)) * QK_LD + fq * 8;
            const char* kxp = smem + L_KX + lane * 16;
#pragma unroll
            for (int ks = 0; ks < 6; ++ks) {
                bf16x8 qf = *reinterpret_cast<const bf16x8*>(qrow + ks * 32);
                s0 = MFMA(*reinterpret_cast<const bf16x8*>(krow + ks * 32), qf, s0);
                s1 = MFMA(*reinterpret_cast<const bf16x8*>(kxp + ks * 1024), qf, s1);
                s2 = MFMA(*reinterpret_cast<const bf16x8*>(kxp + (6 + ks) * 1024), qf, s2);
            }
            float v[12]; float mx = -3.0e38f;
#pragma unroll
            for (int r = 0; r < 4; ++r) {
                v[r] = (fq < 2) ? bfr(bfr(s0[r]) * scale) : -1.0e9f;
                v[4 + r] = bfr(bfr(s1[r]) * scale);
                v[8 + r] = bfr(bfr(s2[r]) * scale);
            }
#pragma unroll
            for (int j = 0; j < 12; ++j) mx = fmaxf(mx, v[j]);
            mx = max4g(mx);
            float sum = 0.f;
#pragma unroll
            for (int j = 0; j < 12; ++j) { v[j] = (j < 4 && fq >= 2) ? 0.f : __expf(v[j] - mx); sum += v[j]; }
            sum = sum4g(sum);
            const float inv = 1.0f / sum;
            if (fr < 8) {
                v4u* pb = a.pg + ((size_t)(s & 1) * NA + h) * 80 + fr * 10;
                const unsigned tg = (unsigned)(s + 1);
                v4u o; o.y = tg; o.w = tg;
                o.x = pack2(v[4] * inv, v[5] * inv); o.z = pack2(v[6] * inv, v[7] * inv);
                st_sc1_x4(pb + 2 + fq, o);
                o.x = pack2(v[8] * inv, v[9] * inv); o.z = pack2(v[10] * inv, v[11] * inv);
                st_sc1_x4(pb + 6 + fq, o);
                if (fq < 2) {
                    o.x = pack2(v[0] * inv, v[1] * inv); o.z = pack2(v[2] * inv, v[3] * inv);
                    st_sc1_x4(pb + fq, o);
                }
            }
            STAMP(4);
            asm volatile("s_waitcnt lgkmcnt(0)" ::: "memory");
            kx_load(s + 1);
        }
        __builtin_amdgcn_s_barrier();
    }
    wg_signal(a.ctl, C_CH + NSEG - 1);
}

template <int T1, int T2, int NL>
__device__ __forceinline__ void role_b_wave(const Mega& a, char* smem, int h, int lane, int t1_0, int t2_0, int lds_off) {
    const int tid = threadIdx.x, fr = lane & 15, fq = lane >> 4;
    constexpr int NF = T1 * 20 + T2 * 6, NREG = NF - NL;
    bf16x8 wr[NREG];
    char* lw = smem + L_W + lds_off + lane * 16;
#pragma unroll
    for (int f = 0; f < NF; ++f) {
        bf16x8 v;
        if (f < T1 * 20) {
            const int t = f / 20, ks = f % 20;
            v = ld_frag(a.WT + (long)(2 * HD + h * DH + (t1_0 + t) * 16 + fr) * DM + ks * 32 + fq * 8);
        } else {
            const int f2 = f - T1 * 20, t = f2 / 6, ks = f2 % 6;
            v = ld_frag(a.WoT + ((long)h * DM + (t2_0 + t) * 16 + fr) * DH + ks * 32 + fq * 8);
        }
        if (f < NREG) wr[f] = v; else *reinterpret_cast<bf16x8*>(lw + (f - NREG) * 1024) = v;
    }
    u16* mimg = reinterpret_cast<u16*>(smem + L_MEM);
    u16* vimg = reinterpret_cast<u16*>(smem + L_QK);
    for (int s = 0; s < NSEG; ++s) {
        if (!gather_mem(a, smem, s, tid)) return;
        __syncthreads();
        {
            f32x4 acc[T1];
#pragma unroll
            for (int t = 0; t < T1; ++t) acc[t] = (f32x4){0.f, 0.f, 0.f, 0.f};
            const u16* mrow = mimg + (lane & 7) * MEM_LD + fq * 8;
#pragma unroll
            for (int ks = 0; ks < 20; ++ks) {
                bf16x8 mf = *reinterpret_cast<const bf16x8*>(mrow + ks * 32);
#pragma unroll
                for (int t = 0; t < T1; ++t) {
                    const int f = t * 20 + ks;
                    bf16x8 w;
                    if (f < NREG) w = wr[f]; else w = *reinterpret_cast<const bf16x8*>(lw + (f - NREG) * 1024);
                    acc[t] = MFMA(w, mf, acc[t]);
                }
            }
            if (fr < 8) {
#pragma unroll
                for (int t = 0; t < T1; ++t) {
                    v2u o; o.x = pack2(acc[t][0], acc[t][1]); o.y = pack2(acc[t][2], acc[t][3]);
                    *reinterpret_cast<v2u*>(vimg + fr * QK_LD + (t1_0 + t) * 16 + fq * 4) = o;
                }
            }
        }
        __syncthreads();
        {
            f32x4 acc[T2];
#pragma unroll
            for (int t = 0; t < T2; ++t) acc[t] = (f32x4){0.f, 0.f, 0.f, 0.f};
            const u16* vrow = vimg + (lane & 7) * QK_LD + fq * 8;
#pragma unroll
            for (int ks = 0; ks < 6; ++ks) {
                bf16x8 vf = *reinterpret_cast<const bf16x8*>(vrow + ks * 32);
#pragma unroll
                for (int t = 0; t < T2; ++t) {
                    const int f = T1 * 20 + t * 6 + ks;
                    bf16x8 w;
                    if (f < NREG) w = wr[f]; else w = *reinterpret_cast<const bf16x8*>(lw + (f - NREG) * 1024);
                    acc[t] = MFMA(vf, w, acc[t]);
                }
            }
            if (fq < 2) {
#pragma unroll
                for (int t = 0; t < T2; ++t) {
                    v4u o; o.x = pack2(acc[t][0], acc[t][1]); o.y = (unsigned)(s + 1); o.z = pack2(acc[t][2], acc[t][3]); o.w = (unsigned)(s + 1);
                    st_sc1_x4(a.zg + (((size_t)(s & 1) * NC + t2_0 + t) * NB + h) * 32 + fr * 2 + fq, o);
                }
            }
        }
    }
}

__device__ __forceinline__ void role_c(const Mega& a, char* smem, int c, int rg) {
    const int tid = threadIdx.x, wave = __builtin_amdgcn_readfirstlane(tid >> 6), lane = tid & 63, fr = lane & 15, fq = lane >> 4;
    u16* pimg = reinterpret_cast<u16*>(smem + LC_P);
    u16* zimg = reinterpret_cast<u16*>(smem + LC_Z);
    float* red = reinterpret_cast<float*>(smem + LC_RED);
    int pdst[3]; unsigned psrc[3];
#pragma unroll
    for (int r = 0; r < 3; ++r) {
        const int idx = (r < 2 || tid < 256) ? tid + 512 * r : tid;
        const int hh = idx / 40, rem = idx % 40, i = rem / 10, j = rem % 10;
        pdst[r] = i * C_PLD + (j < 2 ? 1024 + hh * 8 + 4 * j : hh * 32 + 4 * (j - 2));
        psrc[r] = (unsigned)((hh * 80 + rg * 40 + rem) * 16);
    }
    int zdst[2];
#pragma unroll
    for (int r = 0; r < 2; ++r) {
        const int idx = tid + 512 * r;
        const int hh = idx >> 5, w = idx & 31;
        zdst[r] = (w >> 1) * Z_LD + hh * 8 + (w & 1) * 4;
    }
    const __amdgpu_buffer_rsrc_t urs = __builtin_amdgcn_make_buffer_rsrc((void*)a.UT, 0, NSEG * NH * DM * 32 * 2, 0x00020000);
    bf16x8 uf[4];
    auto u_load = [&](int s) -> bool {
        if (s == 0) { if (!wave_wait(a.ctl, C_U0, 32u)) return false; }
        else if (s == 2) { if (!wave_wait(a.ctl, C_U, 64u)) return false; }
        else if ((s & 3) == 0) { if (!wave_wait(a.ctl, C_U + (s >> 2), 32u)) return false; }
#pragma unroll
        for (int k = 0; k < 4; ++k) {
            const unsigned off = (unsigned)((((s * NH + wave * 4 + k) * DM + c * 16 + fr) * 32 + fq * 8) * 2);
            v4u t = ld_sc1(urs, off);
            uf[k] = __builtin_bit_cast(bf16x8, t);
        }
        return true;
    };
    if (!u_load(0)) return;
    const __amdgpu_buffer_rsrc_t prs = __builtin_amdgcn_make_buffer_rsrc((void*)a.pg, 0, 2 * NA * 80 * 16, 0x00020000);
    const __amdgpu_buffer_rsrc_t zrs = __builtin_amdgcn_make_buffer_rsrc((void*)a.zg, 0, 2 * NC * NB * 32 * 16, 0x00020000);
    constexpr int STAMPW = 0;
    for (int s = 0; s < NSEG; ++s) {
        const bool last = (s + 1 == NSEG);
        const unsigned pb = (unsigned)((s & 1) * (NA * 80) * 16);
        const unsigned zb = (unsigned)((((s & 1) * NC + c) * (NB * 32) + tid) * 16);
        const unsigned tag = (unsigned)(s + 1);
        v4u r0 = (v4u){0u, last ? tag : 0u, 0u, last ? tag : 0u}, r1 = r0, r2 = r0;
        v4u y0 = (v4u){0u, 0u, 0u, 0u}, y1 = y0;
        unsigned spins = 0;
        for (;;) {
            if (!TAGOK(y0, tag)) y0 = ld_sc1(zrs, zb);
            if (!TAGOK(y1, tag)) y1 = ld_sc1(zrs, zb + 512 * 16);
            if (!TAGOK(r0, tag)) r0 = ld_sc1(prs, pb + psrc[0]);
            if (!TAGOK(r1, tag)) r1 = ld_sc1(prs, pb + psrc[1]);
            if (!TAGOK(r2, tag)) r2 = ld_sc1(prs, pb + psrc[2]);
            asm volatile("" ::: "memory");
            bool ok = TAGOK(r0, tag) & TAGOK(r1, tag) & TAGOK(r2, tag) & TAGOK(y0, tag) & TAGOK(y1, tag);
            if (__all(ok)) break;
            if (++spins > SPIN_CAP) { if (lane == 0) set_abort(a.ctl); return; }
            if ((spins & 1023u) == 0u && ld_ctl(a.ctl + CW(C_ABORT)) != 0u) return;
        }
        asm volatile("s_waitcnt vmcnt(0)" ::: "memory");
        STAMP(0);
        auto put8 = [&](u16* dst, v4u r) { v2u o; o.x = r.x; o.y = r.z; *reinterpret_cast<v2u*>(dst) = o; };
        if (!last) {
            put8(pimg + pdst[0], r0); put8(pimg + pdst[1], r1); put8(pimg + pdst[2], r2);
        }
        put8(zimg + zdst[0], y0); put8(zimg + zdst[1], y1);
        __syncthreads();
        STAMP(1);
        if (!last) {
            f32x4 acc = (f32x4){0.f, 0.f, 0.f, 0.f};
            const u16* prow = pimg + (lane & 3) * C_PLD + fq * 8;
#pragma unroll
            for (int k = 0; k < 4; ++k) {
                bf16x8 pf = *reinterpret_cast<const bf16x8*>(prow + wave * 128 + k * 32);
                acc = MFMA(uf[k], pf, acc);
            }
            {
                bf16x8 pf = *reinterpret_cast<const bf16x8*>(prow + 1024 + wave * 32);
                bf16x8 zf = *reinterpret_cast<const bf16x8*>(zimg + fr * Z_LD + wave * 32 + fq * 8);
                acc = MFMA(zf, pf, acc);
            }
            *reinterpret_cast<f32x4*>(red + (wave * 64 + lane) * 4) = acc;
        }
        v4u zo = *reinterpret_cast<const v4u*>(zimg + (tid >> 5) * Z_LD + (tid & 31) * 8);
        __syncthreads();
        if (!last && wave == 0 && fr < 4) {
            f32x4 t = *reinterpret_cast<const f32x4*>(red + lane * 4);
#pragma unroll
            for (int w = 1; w < 8; ++w) {
                f32x4 u = *reinterpret_cast<const f32x4*>(red + (w * 64 + lane) * 4);
                t[0] += u[0]; t[1] += u[1]; t[2] += u[2]; t[3] += u[3];
            }
            v4u o; o.x = pack2(t[0], t[1]); o.y = tag; o.z = pack2(t[2], t[3]); o.w = tag;
            st_sc1_x4(a.memg + ((size_t)((s + 1) & 1) * NC + c) * 32 + (rg * 4 + fr) * 4 + fq, o);
        }
        STAMP(2);
        if (rg == 0) {
            if (s > 0 && tid == 64) __hip_atomic_fetch_add(a.ctl + CW(C_CH + s - 1), 1u, __ATOMIC_RELAXED, __HIP_MEMORY_SCOPE_AGENT);
            st_sc1_x4(a.ZT + ((long)s * DM + c * 16 + (tid >> 5)) * 256 + (tid & 31) * 8, zo);
        }
        if (s + 2 < NSEG) { if (!u_load(s + 1)) return; }
    }
    if (rg == 0) wg_signal(a.ctl, C_CH + NSEG - 1);
}

template <bool FENCE = true>
__device__ __forceinline__ bool wg_wait(const Mega& a, char* smem, int w0, unsigned t0, int w1, unsigned t1, int w2, unsigned t2) {
    volatile int* bc = reinterpret_cast<volatile int*>(smem + L_BCAST);
    if (threadIdx.x < 64) {
        bool ok = wave_wait(a.ctl, w0, t0);
        if (ok && w1 >= 0) ok = wave_wait(a.ctl, w1, t1);
        if (ok && w2 >= 0) ok = wave_wait(a.ctl, w2, t2);
        if constexpr (FENCE) {
            __builtin_amdgcn_fence(__ATOMIC_ACQUIRE, "agent");
            asm volatile("s_waitcnt vmcnt(0)" ::: "memory");
        } else {
            asm volatile("" ::: "memory");
        }
        if (threadIdx.x == 0) bc[1] = ok ? 1 : 0;
    }
    __syncthreads();
    const bool r = bc[1] != 0;
    __syncthreads();
    return r;
}

template <int BM, int BN, int WM, int WN, bool ASC1 = false>
__device__ __forceinline__ void gemm_core(const u16* __restrict__ A, long lda, const u16* __restrict__ B, long ldb, int K, char* sm,
                                          f32x4 (&acc)[WM / 16][WN / 16], const int tid) {
    constexpr int ROWS = BM + BN, TB = ROWS * 128, NWN = BN / WN, L = ROWS / 64;
    static_assert(ROWS % 64 == 0, "rows");
    const int wave = tid >> 6, lane = tid & 63, fr = lane & 15, fq = lane >> 4;
    const int wr = wave / NWN, wc = wave % NWN;
    auto stage = [&](int kt, int buf) {
#pragma unroll
        for (int i = 0; i < L; ++i) {
            const int b = tid * 16 + i * 8192, r = b >> 7, c = ((b >> 4) & 7) ^ (r & 7);
            const u16* src = (r < BM) ? A + (long)r * lda + kt * 64 + c * 8 : B + (long)(r - BM) * ldb + kt * 64 + c * 8;
            if constexpr (ASC1) __builtin_amdgcn_global_load_lds((const unsigned*)src, (unsigned*)(sm + buf * TB + b), 16, 0, 16);
            else __builtin_amdgcn_global_load_lds((const unsigned*)src, (unsigned*)(sm + buf * TB + b), 16, 0, 0);
        }
    };
#pragma unroll
    for (int m = 0; m < WM / 16; ++m)
#pragma unroll
        for (int n = 0; n < WN / 16; ++n) acc[m][n] = (f32x4){0.f, 0.f, 0.f, 0.f};
    const int nt = K >> 6;
    stage(0, 0);
    stage(1, 1);
    int buf = 0;
    const int sw = fr & 7;
    for (int t = 0; t < nt; ++t) {
        if (t + 1 < nt) {
            if constexpr (L == 6) asm volatile("s_waitcnt vmcnt(6)" ::: "memory");
            else if constexpr (L == 5) asm volatile("s_waitcnt vmcnt(5)" ::: "memory");
            else if constexpr (L == 4) asm volatile("s_waitcnt vmcnt(4)" ::: "memory");
            else if constexpr (L == 3) asm volatile("s_waitcnt vmcnt(3)" ::: "memory");
            else asm volatile("s_waitcnt vmcnt(0)" ::: "memory");
        } else {
            asm volatile("s_waitcnt vmcnt(0)" ::: "memory");
        }
        __builtin_amdgcn_s_barrier();
        if (t + 2 < nt) stage(t + 2, buf == 0 ? 2 : buf - 1);
        const char* sa = sm + buf * TB + (wr * WM + fr) * 128;
        const char* sb = sm + buf * TB + (BM + wc * WN + fr) * 128;
#pragma unroll
        for (int kk = 0; kk < 2; ++kk) {
            const int co = ((kk * 4 + fq) ^ sw) * 16;
            bf16x8 af[WM / 16], bfg[WN / 16];
#pragma unroll
            for (int m = 0; m < WM / 16; ++m) af[m] = *reinterpret_cast<const bf16x8*>(sa + m * 2048 + co);
#pragma unroll
            for (int n = 0; n < WN / 16; ++n) bfg[n] = *reinterpret_cast<const bf16x8*>(sb + n * 2048 + co);
#pragma unroll
            for (int m = 0; m < WM / 16; ++m)
#pragma unroll
                for (int n = 0; n < WN / 16; ++n) acc[m][n] = MFMA(bfg[n], af[m], acc[m][n]);
        }
        buf = (buf == 2) ? 0 : buf + 1;
    }
    asm volatile("s_waitcnt lgkmcnt(0)" ::: "memory");
    __syncthreads();
}

__device__ __forceinline__ void task_qk(const Mega& a, char* smem, int rb, int col0, const int tid) {
    f32x4 acc[4][4];
    gemm_core<128, 256, 64, 64>(a.xn + (long)rb * 128 * DM, DM, a.WT + (long)col0 * DM, DM, DM, smem, acc, tid);
    const int wave = tid >> 6, lane = tid & 63, fr = lane & 15, fq = lane >> 4, wr = wave >> 2, wc = wave & 3;
    u16* st = reinterpret_cast<u16*>(smem);
#pragma unroll
    for (int m = 0; m < 4; ++m)
#pragma unroll
        for (int n = 0; n < 4; ++n) {
            v2u o; o.x = pack2(acc[m][n][0], acc[m][n][1]); o.y = pack2(acc[m][n][2], acc[m][n][3]);
            *reinterpret_cast<v2u*>(st + (wr * 64 + m * 16 + fr) * 264 + wc * 64 + n * 16 + fq * 4) = o;
        }
    __syncthreads();
#pragma unroll
    for (int i = 0; i < 8; ++i) {
        const int id = tid + 512 * i, row = id >> 5, c16 = id & 31;
        v4u v = *reinterpret_cast<const v4u*>(st + row * 264 + c16 * 8);
        st_sc1_x4(a.QK + (long)(rb * 128 + row) * QLD + col0 + c16 * 8, v);
    }
}

#define VU_V 0
#define VU_U (52 * 1024)
template <int NSG>
__device__ __forceinline__ void task_vu(const Mega& a, char* smem, int seg0, int h, const int tid) {
    const int wave = tid >> 6, lane = tid & 63, fr = lane & 15, fq = lane >> 4;
    u16* vimg = reinterpret_cast<u16*>(smem + VU_V);
    u16* ust = reinterpret_cast<u16*>(smem + VU_U);
    {
        f32x4 acc[NSG][3];
        gemm_core<32 * NSG, 192, 16 * NSG, 48>(a.xn + (long)seg0 * 32 * DM, DM, a.WT + (long)(2 * HD + h * DH) * DM, DM, DM, smem, acc, tid);
        const int wr = wave >> 2, wc = wave & 3;
#pragma unroll
        for (int m = 0; m < NSG; ++m)
#pragma unroll
            for (int n = 0; n < 3; ++n) {
                v2u o; o.x = pack2(acc[m][n][0], acc[m][n][1]); o.y = pack2(acc[m][n][2], acc[m][n][3]);
                *reinterpret_cast<v2u*>(vimg + (wr * 16 * NSG + m * 16 + fr) * 200 + wc * 48 + n * 16 + fq * 4) = o;
            }
    }
    bf16x8 wof[5][6];
#pragma unroll
    for (int ct = 0; ct < 5; ++ct)
#pragma unroll
        for (int ks = 0; ks < 6; ++ks)
            wof[ct][ks] = ld_frag(a.WoT + ((long)h * DM + (wave * 5 + ct) * 16 + fr) * DH + ks * 32 + fq * 8);
    __syncthreads();
    for (int sg = 0; sg < NSG; ++sg) {
        f32x4 acc[5][2];
#pragma unroll
        for (int ct = 0; ct < 5; ++ct) { acc[ct][0] = (f32x4){0.f, 0.f, 0.f, 0.f}; acc[ct][1] = acc[ct][0]; }
        const u16* v0 = vimg + (sg * 32 + fr) * 200 + fq * 8;
#pragma unroll
        for (int ks = 0; ks < 6; ++ks) {
            bf16x8 a0 = *reinterpret_cast<const bf16x8*>(v0 + ks * 32);
            bf16x8 a1 = *reinterpret_cast<const bf16x8*>(v0 + 16 * 200 + ks * 32);
#pragma unroll
            for (int ct = 0; ct < 5; ++ct) { acc[ct][0] = MFMA(a0, wof[ct][ks], acc[ct][0]); acc[ct][1] = MFMA(a1, wof[ct][ks], acc[ct][1]); }
        }
#pragma unroll
        for (int ct = 0; ct < 5; ++ct)
#pragma unroll
            for (int rt = 0; rt < 2; ++rt) {
                v2u o; o.x = pack2(acc[ct][rt][0], acc[ct][rt][1]); o.y = pack2(acc[ct][rt][2], acc[ct][rt][3]);
                *reinterpret_cast<v2u*>(ust + ((wave * 5 + ct) * 16 + fr) * 32 + rt * 16 + fq * 4) = o;
            }
        __syncthreads();
        u16* dst = a.UT + ((long)(seg0 + sg) * NH + h) * (DM * 32);
#pragma unroll
        for (int i = 0; i < 5; ++i) {
            const int id = tid + 512 * i;
            v4u v = *reinterpret_cast<const v4u*>(ust + id * 8);
            st_sc1_x4(dst + id * 8, v);
        }
        __syncthreads();
    }
}

__device__ __forceinline__ bool task_p(const Mega& a, char* smem, int seg, int hg, const int tid) {
    const int wave = tid >> 6, lane = tid & 63, fr = lane & 15, fq = lane >> 4;
    const float scale = 0.07216878364870322f;
    const int h = hg * 8 + wave;
    const u16* kx0 = a.QK + (long)(seg * 32 + fr) * QLD + HD + h * DH + fq * 8;
    const u16* qp = a.QK + (long)(seg * 32 + fr) * QLD + h * DH + fq * 8;
    if (!wg_wait(a, smem, C_Q + (seg >> 2), 24u, C_K + (seg >> 2), 24u, -1, 0u)) return false;
    bf16x8 kmf[6], kf0[6], kf1[6], qf[2][6];
#pragma unroll
    for (int ks = 0; ks < 6; ++ks) {
        qf[0][ks] = ld_frag(qp + ks * 32); kf0[ks] = ld_frag(kx0 + ks * 32);
        kf1[ks] = ld_frag(kx0 + (long)16 * QLD + ks * 32); qf[1][ks] = ld_frag(qp + (long)16 * QLD + ks * 32);
    }
    f32x4 sx[3];
    sx[0] = (f32x4){0.f, 0.f, 0.f, 0.f}; sx[1] = sx[0]; sx[2] = sx[0];
#pragma unroll
    for (int ks = 0; ks < 6; ++ks) {
        sx[0] = MFMA(kf0[ks], qf[0][ks], sx[0]);
        sx[1] = MFMA(kf0[ks], qf[1][ks], sx[1]);
        sx[2] = MFMA(kf1[ks], qf[1][ks], sx[2]);
    }
    if (!wg_wait<false>(a, smem, C_CH + seg, (unsigned)(NA + NC), -1, 0u, -1, 0u)) return false;
#pragma unroll
    for (int ks = 0; ks < 6; ++ks) kmf[ks] = __builtin_bit_cast(bf16x8, ld_sc1p(a.km, (unsigned)(((seg * NH + h) * 8 + (fr & 7)) * DH + fq * 8 + ks * 32) * 2));
#pragma unroll
    for (int qt = 0; qt < 2; ++qt) {
        f32x4 s0 = (f32x4){0.f, 0.f, 0.f, 0.f};
        const f32x4 s1 = sx[qt], s2 = sx[2];
#pragma unroll
        for (int ks = 0; ks < 6; ++ks) s0 = MFMA(kmf[ks], qf[qt][ks], s0);
        const int ql = qt * 16 + fr;
        float v[12]; float mx = -3.0e38f;
#pragma unroll
        for (int r = 0; r < 4; ++r) {
            v[r] = (fq < 2) ? bfr(bfr(s0[r]) * scale) : -1.0e9f;
            v[4 + r] = (fq * 4 + r <= ql) ? bfr(bfr(s1[r]) * scale) : -1.0e9f;
            v[8 + r] = (16 + fq * 4 + r <= ql) ? bfr(bfr(s2[r]) * scale) : -1.0e9f;
        }
#pragma unroll
        for (int j = 0; j < 12; ++j) mx = fmaxf(mx, v[j]);
        mx = max4g(mx);
        float sum = 0.f;
#pragma unroll
        for (int j = 0; j < 12; ++j) { v[j] = (j < 4 && fq >= 2) ? 0.f : __expf(v[j] - mx); sum += v[j]; }
        sum = sum4g(sum);
        const float inv = 1.0f / sum;
        u16* prow = a.Pbuf + ((long)seg * 32 + ql) * 1280;
        if (fq < 2) {
            v2u o; o.x = pack2(v[0] * inv, v[1] * inv); o.y = pack2(v[2] * inv, v[3] * inv);
            st_sc1_x2(prow + 1024 + h * 8 + fq * 4, o);
        }
        v2u o; o.x = pack2(v[4] * inv, v[5] * inv); o.y = pack2(v[6] * inv, v[7] * inv);
        st_sc1_x2(prow + h * 32 + fq * 4, o);
        o.x = pack2(v[8] * inv, v[9] * inv); o.y = pack2(v[10] * inv, v[11] * inv);
        st_sc1_x2(prow + h * 32 + 16 + fq * 4, o);
    }
    return true;
}

#define XP_LD 1288
__device__ __forceinline__ bool task_o(const Mega& a, char* smem, int seg, int cb, const int tid) {
    u16* P = reinterpret_cast<u16*>(smem);
    const int wave = tid >> 6, lane = tid & 63, fr = lane & 15, fq = lane >> 4;
    const int col0 = cb * 128 + wave * 16;
    f32x4 a0 = (f32x4){0.f, 0.f, 0.f, 0.f}, a1 = a0;
    const u16* up = a.UT + ((long)seg * NH * DM + col0 + fr) * 32 + fq * 8;
    if (!wg_wait(a, smem, C_U + (seg >> 2), seg < 4 ? 64u : 32u, -1, 0u, -1, 0u)) return false;
    bf16x8 uf[32];
#pragma unroll
    for (int k = 0; k < 32; ++k) uf[k] = ld_frag(up + (long)k * (DM * 32));
    if (!wg_wait<false>(a, smem, C_P + seg, 4u, -1, 0u, -1, 0u)) return false;
    bf16x8 zf[8];
#pragma unroll
    for (int k = 0; k < 8; ++k) zf[k] = __builtin_bit_cast(bf16x8, ld_sc1p(a.ZT, (unsigned)(((seg * DM + col0 + fr) * 256 + fq * 8 + k * 32) * 2)));
#pragma unroll
    for (int i = 0; i < 10; ++i) {
        const int id = tid + 512 * i, row = id / 160, c = (id % 160) * 8;
        *reinterpret_cast<v4u*>(P + row * XP_LD + c) = ld_sc1p(a.Pbuf, (unsigned)(((seg * 32 + row) * 1280 + c) * 2));
    }
    __syncthreads();
    const u16* p0 = P + fr * XP_LD + fq * 8;
    const u16* p1 = P + (16 + fr) * XP_LD + fq * 8;
#pragma unroll
    for (int ks = 0; ks < 32; ++ks) {
        a0 = MFMA(uf[ks], *reinterpret_cast<const bf16x8*>(p0 + ks * 32), a0);
        a1 = MFMA(uf[ks], *reinterpret_cast<const bf16x8*>(p1 + ks * 32), a1);
    }
#pragma unroll
    for (int ks = 0; ks < 8; ++ks) {
        a0 = MFMA(zf[ks], *reinterpret_cast<const bf16x8*>(p0 + 1024 + ks * 32), a0);
        a1 = MFMA(zf[ks], *reinterpret_cast<const bf16x8*>(p1 + 1024 + ks * 32), a1);
    }
#pragma unroll
    for (int qt = 0; qt < 2; ++qt) {
        const f32x4 av = qt ? a1 : a0;
        const long off = (long)(seg * 32 + qt * 16 + fr) * DM + col0 + fq * 4;
        v2u xi = *reinterpret_cast<const v2u*>(a.x + off);
        v2u o;
        o.x = pack2(bf2f((u16)(xi.x & 0xffff)) + bfr(av[0]), bf2f((u16)(xi.x >> 16)) + bfr(av[1]));
        o.y = pack2(bf2f((u16)(xi.y & 0xffff)) + bfr(av[2]), bf2f((u16)(xi.y >> 16)) + bfr(av[3]));
        st_sc1_x2(a.hbuf + off, o);
    }
    return true;
}

__device__ __forceinline__ float gelu_tanh(float x) {
    const float u = 0.7978845608028654f * (x + 0.044715f * x * x * x);
    return __fdividef(x, 1.0f + __expf(-2.0f * u));
}
#define M1_A 0
#define M1_B (83 * 1024)
#define M1_ST 0
__device__ __forceinline__ bool task_mlp1(const Mega& a, char* smem, int b64, int ct, const int tid) {
    const int wave = tid >> 6, lane = tid & 63, fr = lane & 15, fq = lane >> 4, wr = wave >> 2, wc = wave & 3;
    u16* aimg = reinterpret_cast<u16*>(smem + M1_A);
    const u16* Bg = a.w1T + (long)ct * 128 * DM;
    auto stage = [&](int kt) {
        const int b = tid * 16, r = b >> 6, c = ((b >> 4) & 3) ^ ((r >> 2) & 3);
        __builtin_amdgcn_global_load_lds((const unsigned*)(Bg + (long)r * DM + kt * 32 + c * 8), (unsigned*)(smem + M1_B + (kt & 7) * 8192 + b), 16, 0, 0);
    };
#pragma unroll
    for (int i = 0; i < 7; ++i) stage(i);
    if (!wg_wait<false>(a, smem, C_X + b64, 10u, -1, 0u, -1, 0u)) return false;
#pragma unroll
    for (int i = 0; i < 10; ++i) {
        const int id = tid + 512 * i, row = id / 80, c = (id % 80) * 8;
        *reinterpret_cast<v4u*>(aimg + row * MEM_LD + c) = ld_sc1p(a.hbuf, (unsigned)(((b64 * 64 + row) * DM + c) * 2));
    }
    __syncthreads();
#pragma unroll
    for (int ps = 0; ps < 2; ++ps) {
        u16* rp = aimg + (wave * 8 + ps * 4 + fq) * MEM_LD + fr * 8;
        float v[40];
#pragma unroll
        for (int j = 0; j < 5; ++j) {
            v4u t = *reinterpret_cast<const v4u*>(rp + j * 128);
            v[j * 8 + 0] = __uint_as_float(t.x << 16); v[j * 8 + 1] = __uint_as_float(t.x & 0xffff0000u);
            v[j * 8 + 2] = __uint_as_float(t.y << 16); v[j * 8 + 3] = __uint_as_float(t.y & 0xffff0000u);
            v[j * 8 + 4] = __uint_as_float(t.z << 16); v[j * 8 + 5] = __uint_as_float(t.z & 0xffff0000u);
            v[j * 8 + 6] = __uint_as_float(t.w << 16); v[j * 8 + 7] = __uint_as_float(t.w & 0xffff0000u);
        }
#pragma unroll
        for (int rep = 0; rep < 2; ++rep) {
            float sm = 0.f;
#pragma unroll
            for (int i = 0; i < 40; ++i) sm += v[i];
            sm += __shfl_xor(sm, 1); sm += __shfl_xor(sm, 2); sm += __shfl_xor(sm, 4); sm += __shfl_xor(sm, 8);
            const float mean = sm * (1.0f / DM);
            float q = 0.f;
#pragma unroll
            for (int i = 0; i < 40; ++i) { const float d = v[i] - mean; q += d * d; }
            q += __shfl_xor(q, 1); q += __shfl_xor(q, 2); q += __shfl_xor(q, 4); q += __shfl_xor(q, 8);
            const float mb = bfr(mean);
            const float rden = 1.0f / bfr(sqrtf(bfr(bfr(q * (1.0f / DM)) + 1e-5f)));
#pragma unroll
            for (int i = 0; i < 40; ++i) v[i] = bfr(bfr(v[i] - mb) * rden);
        }
#pragma unroll
        for (int j = 0; j < 5; ++j) {
            v4u t;
            t.x = pack2(v[j * 8 + 0], v[j * 8 + 1]); t.y = pack2(v[j * 8 + 2], v[j * 8 + 3]);
            t.z = pack2(v[j * 8 + 4], v[j * 8 + 5]); t.w = pack2(v[j * 8 + 6], v[j * 8 + 7]);
            *reinterpret_cast<v4u*>(rp + j * 128) = t;
        }
    }
    f32x4 acc[2][2];
#pragma unroll
    for (int m = 0; m < 2; ++m)
#pragma unroll
        for (int n = 0; n < 2; ++n) acc[m][n] = (f32x4){0.f, 0.f, 0.f, 0.f};
    const int sw = (fq ^ ((fr >> 2) & 3)) * 16;
    for (int t = 0; t < 20; ++t) {
        if (t + 6 < 20) asm volatile("s_waitcnt vmcnt(6) lgkmcnt(0)" ::: "memory");
        else asm volatile("s_waitcnt vmcnt(0) lgkmcnt(0)" ::: "memory");
        __builtin_amdgcn_s_barrier();
        if (t + 7 < 20) stage(t + 7);
        bf16x8 af[2], bfg[2];
#pragma unroll
        for (int m = 0; m < 2; ++m) af[m] = *reinterpret_cast<const bf16x8*>(aimg + (wr * 32 + m * 16 + fr) * MEM_LD + t * 32 + fq * 8);
#pragma unroll
        for (int n = 0; n < 2; ++n) bfg[n] = *reinterpret_cast<const bf16x8*>(smem + M1_B + (t & 7) * 8192 + (wc * 32 + n * 16 + fr) * 64 + sw);
#pragma unroll
        for (int m = 0; m < 2; ++m)
#pragma unroll
            for (int n = 0; n < 2; ++n) acc[m][n] = MFMA(bfg[n], af[m], acc[m][n]);
    }
    asm volatile("s_waitcnt lgkmcnt(0)" ::: "memory");
    __syncthreads();
    u16* st = reinterpret_cast<u16*>(smem + M1_ST);
#pragma unroll
    for (int n = 0; n < 2; ++n) {
        const int cl = wc * 32 + n * 16 + fq * 4;
        v2u bb = *reinterpret_cast<const v2u*>(a.b1 + ct * 128 + cl);
        const float b0 = bf2f((u16)(bb.x & 0xffff)), b1 = bf2f((u16)(bb.x >> 16)), b2 = bf2f((u16)(bb.y & 0xffff)), b3 = bf2f((u16)(bb.y >> 16));
#pragma unroll
        for (int m = 0; m < 2; ++m) {
            v2u o;
            o.x = pack2(gelu_tanh(bfr(bfr(acc[m][n][0]) + b0)), gelu_tanh(bfr(bfr(acc[m][n][1]) + b1)));
            o.y = pack2(gelu_tanh(bfr(bfr(acc[m][n][2]) + b2)), gelu_tanh(bfr(bfr(acc[m][n][3]) + b3)));
            *reinterpret_cast<v2u*>(st + (wr * 32 + m * 16 + fr) * 136 + cl) = o;
        }
    }
    __syncthreads();
#pragma unroll
    for (int i = 0; i < 2; ++i) {
        const int id = tid + 512 * i, row = id >> 4, c16 = id & 15;
        v4u v = *reinterpret_cast<const v4u*>(st + row * 136 + c16 * 8);
        st_sc1_x4(a.hid + (long)(b64 * 64 + row) * FF + ct * 128 + c16 * 8, v);
    }
    return true;
}

__device__ __forceinline__ void task_mlp2(const Mega& a, char* smem, int b64, int ct, const int tid) {
    f32x4 acc[2][2];
    gemm_core<64, 128, 32, 32, true>(a.hid + (long)b64 * 64 * FF, FF, a.w2T + (long)ct * 128 * FF, FF, FF, smem, acc, tid);
    const int wave = tid >> 6, lane = tid & 63, fr = lane & 15, fq = lane >> 4, wr = wave >> 2, wc = wave & 3;
#pragma unroll
    for (int n = 0; n < 2; ++n) {
        const int col = ct * 128 + wc * 32 + n * 16 + fq * 4;
        v2u bb = *reinterpret_cast<const v2u*>(a.b2 + col);
        const float b0 = bf2f((u16)(bb.x & 0xffff)), b1 = bf2f((u16)(bb.x >> 16)), b2 = bf2f((u16)(bb.y & 0xffff)), b3 = bf2f((u16)(bb.y >> 16));
#pragma unroll
        for (int m = 0; m < 2; ++m) {
            const long off = (long)(b64 * 64 + wr * 32 + m * 16 + fr) * DM + col;
            v2u hi = ld_sc1p2(a.hbuf, (unsigned)(off * 2));
            v2u o;
            o.x = pack2(bf2f((u16)(hi.x & 0xffff)) + bfr(bfr(acc[m][n][0]) + b0), bf2f((u16)(hi.x >> 16)) + bfr(bfr(acc[m][n][1]) + b1));
            o.y = pack2(bf2f((u16)(hi.y & 0xffff)) + bfr(bfr(acc[m][n][2]) + b2), bf2f((u16)(hi.y >> 16)) + bfr(bfr(acc[m][n][3]) + b3));
            *reinterpret_cast<v2u*>(a.out + off) = o;
        }
    }
}

#define T_P1 928
#define T_P2 (T_P1 + 384)
#define T_END (T_P2 + 35 * 35)
__device__ __forceinline__ void worker(const Mega& a, char* smem) {
    volatile int* bc = reinterpret_cast<volatile int*>(smem + L_BCAST);
    for (;;) {
        if (threadIdx.x == 0) bc[0] = (int)__hip_atomic_fetch_add(a.ctl + CW(C_TICKET), 1u, __ATOMIC_RELAXED, __HIP_MEMORY_SCOPE_AGENT);
        __syncthreads();
        const int t = __builtin_amdgcn_readfirstlane(bc[0]);
        __syncthreads();
        if (t >= T_END) return;
        int tid = threadIdx.x;
        asm volatile("" : "+v"(tid));
        if (t < T_P1) {
            if (t < 88) {
                if (t < 32) { task_vu<2>(a, smem, 0, t, tid); wg_signal(a.ctl, C_U0); if (threadIdx.x == 0) __hip_atomic_fetch_add(a.ctl + CW(C_U), 1u, __ATOMIC_RELAXED, __HIP_MEMORY_SCOPE_AGENT); }
                else if (t < 56) { task_qk(a, smem, 0, HD + (t - 32) * 256, tid); wg_signal(a.ctl, C_K); }
                else { task_vu<2>(a, smem, 2, t - 56, tid); wg_signal(a.ctl, C_U); }
            } else {
                const int rb = (t - 32) / 56, r = (t - 32) % 56;
                if (r < 32) { task_vu<4>(a, smem, rb * 4, r, tid); wg_signal(a.ctl, C_U + rb); }
                else { task_qk(a, smem, rb, HD + (r - 32) * 256, tid); wg_signal(a.ctl, C_K + rb); }
            }
        } else if (t < T_P2) {
            const int u = t - T_P1, rb = u / 24, ct = u % 24;
            task_qk(a, smem, rb, ct * 256, tid); wg_signal(a.ctl, C_Q + rb);
        } else {
            const int u = t - T_P2, g = u / 35, r = u % 35;
            if (r < 8) {
                const int b = g;
                if (b < 32) {
                    const int seg = 2 * b + (r >> 2);
                    if (!task_p(a, smem, seg, r & 3, tid)) return;
                    wg_signal(a.ctl, C_P + seg);
                }
            } else if (r < 18) {
                const int b = g - 1;
                if (b >= 0 && b < 32) {
                    const int seg = 2 * b + (r - 8) / 5;
                    if (!task_o(a, smem, seg, (r - 8) % 5, tid)) return;
                    wg_signal(a.ctl, C_X + b);
                }
            } else if (r < 30) {
                const int b = g - 2;
                if (b >= 0 && b < 32) {
                    if (!task_mlp1(a, smem, b, r - 18, tid)) return;
                    wg_signal(a.ctl, C_M + b);
                }
            } else {
                const int b = g - 3;
                if (b >= 0 && b < 32) {
                    if (!wg_wait<false>(a, smem, C_M + b, 12u, -1, 0u, -1, 0u)) return;
                    task_mlp2(a, smem, b, r - 30, tid);
                }
            }
        }
        __syncthreads();
    }
}

__global__ __launch_bounds__(512, 2) void k_mega(Mega a) {
    __shared__ __attribute__((aligned(16))) char smem[160 * 1024];
    const int b = blockIdx.x;
    if (b < NA) {
        role_a(a, smem, b);
    } else if (b < NA + NB) {
        const int h = b - NA;
        const int wave = __builtin_amdgcn_readfirstlane(threadIdx.x >> 6), lane = threadIdx.x & 63;
        if (wave < 4) role_b_wave<2, 4, 21>(a, smem, h, lane, 2 * wave, 4 * wave, wave * 21 * 1024);
        else role_b_wave<1, 6, 13>(a, smem, h, lane, 8 + (wave - 4), 16 + 6 * (wave - 4), (84 + (wave - 4) * 13) * 1024);
    } else if (b < NCHAIN) {
        role_c(a, smem, (b - NA - NB) >> 1, (b - NA - NB) & 1);
    } else {
        worker(a, smem);
    }
}

extern "C" void kernel_launch(void* const* d_in, const int* in_sizes, int n_in, void* d_out, int out_size, void* d_ws,
                              size_t ws_size, hipStream_t stream) {
    char* ws = (char*)d_ws;
    size_t off = 0;
    auto take = [&](size_t bytes) { char* p = ws + off; off += (bytes + 255) & ~(size_t)255; return p; };
    const size_t gran_bytes = (size_t)(2 * NC * 32 + 2 * NA * 80 + 2 * NC * NB * 32) * 16;
    const size_t comm_bytes = gran_bytes + (size_t)C_NWORDS * 64;
    char* comm = take(comm_bytes);
    u16* xn = (u16*)take((size_t)NTOK * DM * 2);
    u16* WT = (u16*)take((size_t)3 * HD * DM * 2);
    u16* WoT = (u16*)take((size_t)NH * DM * DH * 2);
    u16* w1T = (u16*)take((size_t)FF * DM * 2);
    u16* w2T = (u16*)take((size_t)DM * FF * 2);
    u16* QK = (u16*)take((size_t)NTOK * QLD * 2);
    u16* UT = (u16*)take((size_t)NSEG * NH * DM * 32 * 2);
    u16* km = (u16*)take((size_t)NSEG * NH * 8 * DH * 2);
    u16* ZT = (u16*)take((size_t)NSEG * DM * 256 * 2);
    u16* hbuf = (u16*)take((size_t)NTOK * DM * 2);
    u16* hid = (u16*)take((size_t)NTOK * FF * 2);
    u16* Pbuf = (u16*)take((size_t)NTOK * 1280 * 2);

    PreArgs p;
    p.Wq = (const u16*)d_in[2]; p.Wk = (const u16*)d_in[3]; p.Wv = (const u16*)d_in[4]; p.Wo = (const u16*)d_in[5];
    p.w1 = (const u16*)d_in[8]; p.w2 = (const u16*)d_in[10]; p.x = (const u16*)d_in[0];
    p.WT = WT; p.WoT = WoT; p.w1T = w1T; p.w2T = w2T; p.xn = xn;
    p.zero = (v4u*)comm; p.nzero16 = (long)(comm_bytes / 16);
    k_pre<<<PRE_BLOCKS, 256, 0, stream>>>(p);

    Mega a;
    a.x = (const u16*)d_in[0]; a.mem0 = (const u16*)d_in[1]; a.b1 = (const u16*)d_in[9]; a.b2 = (const u16*)d_in[11];
    a.xn = xn; a.WT = WT; a.WoT = WoT; a.w1T = w1T; a.w2T = w2T;
    a.QK = QK; a.UT = UT; a.km = km; a.ZT = ZT; a.hbuf = hbuf; a.hid = hid; a.out = (u16*)d_out; a.Pbuf = Pbuf;
    a.memg = (v4u*)comm; a.pg = a.memg + 2 * NC * 32; a.zg = a.pg + 2 * NA * 80;
    a.ctl = (unsigned*)(comm + gran_bytes);
    k_mega<<<256, 512, 0, stream>>>(a);
}
```
